```python
import torch

def _softmax(x, axis=-1):
    return torch.softmax(x, dim=axis)

def _gelu(x):
    return torch.nn.functional.gelu(x, approximate='tanh')

def _silu(x):
    return torch.nn.functional.silu(x)

def _sigmoid(x):
    return torch.sigmoid(x)

def _layernorm(x, axis=-1, eps=1e-05):
    m = x.mean(dim=axis, keepdim=True)
    v = x.var(dim=axis, keepdim=True, unbiased=False)
    return (x - m) / torch.sqrt(v + eps)

def _scatter_add(tgt, idx, src, axis=0):
    return tgt.clone().index_add_(axis, idx, src)

def _segment_sum(src, seg, n):
    out = torch.zeros((n,) + src.shape[1:], dtype=src.dtype)
    return out.index_add_(0, seg, src)

def _pad(x, pw):
    flat = tuple((v for pair in reversed(pw) for v in pair))
    return torch.nn.functional.pad(x, flat)
kernel_name = 'parallel_sum__memory_aug_recurrent_mem_tr__glu_gated_glu__prompt'

def setup_inputs():

    _w = lambda a: (lambda t: t.to(torch.bfloat16) if t.is_floating_point() else t)(torch.as_tensor(a))

    def _A_setup():
        B, S, Dm = (1, 2048, 512)
        H, Dh = (32, 128)
        M, seg = (8, 32)
        seg = min(seg, S)
        S = S // seg * seg
        return {'x': _w((lambda *s: torch.randn(*s, dtype=torch.bfloat16) * 0.05)(B, S, Dm)), 'mem0': _w((lambda *s: torch.randn(*s, dtype=torch.bfloat16) * 1.6)(B, M, Dm)), 'Wq': _w((lambda *s: torch.randn(*s, dtype=torch.bfloat16) * 0.8)(Dm, H, Dh) * Dm ** (-0.5)), 'Wk': _w((lambda *s: torch.randn(*s, dtype=torch.bfloat16) * 0.8)(Dm, H, Dh) * Dm ** (-0.5)), 'Wv': _w((lambda *s: torch.randn(*s, dtype=torch.bfloat16) * 1.6)(Dm, H, Dh) * Dm ** (-0.5)), 'Wo': _w((lambda *s: torch.randn(*s, dtype=torch.bfloat16) * 1.6)(H, Dh, Dm) * (H * Dh) ** (-0.5)), 'seg_len': seg}
    _A_in = _A_setup()

    def _B_setup():
        B, S, D, F = (1, 2048, 512, 1792)
        return {'x': _w((lambda *s: torch.randn(*s, dtype=torch.bfloat16) * 0.05)(B, S, D)), 'w_gate': _w((lambda *s: torch.randn(*s, dtype=torch.bfloat16) * 0.05)(D, F)), 'w_up': _w((lambda *s: torch.randn(*s, dtype=torch.bfloat16) * 0.05)(D, F)), 'w_down': _w((lambda *s: torch.randn(*s, dtype=torch.bfloat16) * 0.05)(F, D))}
    _B_in = _B_setup()
    return {'A_x': _A_in['x'], 'A_mem0': _A_in['mem0'], 'A_Wq': _A_in['Wq'], 'A_Wk': _A_in['Wk'], 'A_Wv': _A_in['Wv'], 'A_Wo': _A_in['Wo'], 'A_seg_len': _A_in['seg_len'], 'B_x': _B_in['x'], 'B_w_gate': _B_in['w_gate'], 'B_w_up': _B_in['w_up'], 'B_w_down': _B_in['w_down']}

def reference(A_x, A_mem0, A_Wq, A_Wk, A_Wv, A_Wo, A_seg_len, B_x, B_w_gate, B_w_up, B_w_down):

    def _A_ref(x, mem0, Wq, Wk, Wv, Wo, seg_len):
        B, S, Dm = x.shape
        H, Dh = (Wq.shape[1], Wq.shape[2])
        M = mem0.shape[1]
        scale = Dh ** (-0.5)
        n_seg = S // seg_len
        L = M + seg_len
        idx = torch.arange(L)
        mask = (idx[:, None] < M) | (idx[:, None] >= idx[None, :])
        mem = mem0
        outs = []
        for s in range(n_seg):
            xs = x[:, s * seg_len:(s + 1) * seg_len, :].clone()
            h = torch.concatenate([mem, xs], axis=1)
            q = torch.einsum('btd,dhe->bhte', h, Wq)
            k = torch.einsum('btd,dhe->bhte', h, Wk)
            v = torch.einsum('btd,dhe->bhte', h, Wv)
            scores = torch.einsum('bhqe,bhke->bhqk', q, k) * scale
            scores = torch.where(mask[None, None], scores, -1000000000.0)
            p = _softmax(scores, axis=-1)
            o = torch.einsum('bhqk,bhke->bhqe', p, v)
            o = torch.einsum('bhte,hed->btd', o, Wo)
            mem = o[:, :M, :].clone()
            outs.append(o[:, M:, :])
        return torch.concatenate(outs, axis=1)

    def _B_ref(x, w_gate, w_up, w_down):
        g = torch.einsum('bsd,df->bsf', x, w_gate)
        u = torch.einsum('bsd,df->bsf', x, w_up)
        h = _sigmoid(g) * u
        return torch.einsum('bsf,fd->bsd', h, w_down)
    x = A_x
    xn = _layernorm(x, axis=-1)
    return x + _A_ref(xn, A_mem0, A_Wq, A_Wk, A_Wv, A_Wo, A_seg_len) + _B_ref(xn, B_w_gate, B_w_up, B_w_down)
```

```cpp
#include <hip/hip_runtime.h>
#include <hip/hip_bf16.h>

constexpr int S_ = 2048, DM = 512, NH = 32, DH = 128, MT = 8, SEG = 32, NSEG = 64, FF = 1792;

typedef unsigned short u16;
typedef __attribute__((ext_vector_type(8))) short bf16x8;
typedef __attribute__((ext_vector_type(4))) float f32x4;
typedef __attribute__((ext_vector_type(4))) unsigned v4u;
typedef __attribute__((ext_vector_type(2))) unsigned v2u;
typedef __bf16 bf16x2_t __attribute__((ext_vector_type(2)));

__device__ __forceinline__ unsigned pack2(float a, float b) {
    bf16x2_t v = {(__bf16)a, (__bf16)b};
    return __builtin_bit_cast(unsigned, v);
}
__device__ __forceinline__ float rbf(float a) { return (float)(__bf16)a; }
__device__ __forceinline__ float bf2f(u16 b) { return __uint_as_float(((unsigned)b) << 16); }
__device__ __forceinline__ bf16x8 mk8(unsigned a, unsigned b, unsigned c, unsigned d) {
    v4u v = {a, b, c, d};
    return __builtin_bit_cast(bf16x8, v);
}
#define MFMA(a, b, c) __builtin_amdgcn_mfma_f32_16x16x32_bf16((a), (b), (c), 0, 0, 0)

__device__ __forceinline__ int pi_col(int tau, int rho) { return 32 * (tau >> 1) + 8 * (rho >> 2) + 4 * (tau & 1) + (rho & 3); }

constexpr size_t OFF_TMO = 0;
constexpr size_t OFF_M   = 256;
constexpr size_t OFF_P   = OFF_M + 64 * 8192;
constexpr size_t ZERO_BYTES = OFF_P + 32 * 16384;
constexpr size_t OFF_XN  = (ZERO_BYTES + 4095) / 4096 * 4096;
constexpr size_t OFF_WT  = OFF_XN + (size_t)S_ * DM * 2;
constexpr size_t OFF_WOT = OFF_WT + (size_t)3 * DM * NH * DH * 2;
constexpr size_t OFF_WG  = OFF_WOT + (size_t)DM * NH * DH * 2;
constexpr size_t OFF_WU  = OFF_WG + (size_t)DM * FF * 2;
constexpr size_t OFF_WD  = OFF_WU + (size_t)DM * FF * 2;
constexpr size_t OFF_QX  = OFF_WD + (size_t)DM * FF * 2;
constexpr size_t OFF_KX  = OFF_QX + (size_t)S_ * NH * DH * 2;
constexpr size_t OFF_VXT = OFF_KX + (size_t)S_ * NH * DH * 2;
constexpr size_t OFF_KM  = OFF_VXT + (size_t)S_ * NH * DH * 2;
constexpr size_t OFF_VM  = OFF_KM + (size_t)NSEG * NH * MT * DH * 2;
constexpr size_t OFF_H   = OFF_VM + (size_t)NSEG * NH * MT * DH * 2;
constexpr size_t OFF_O   = OFF_H + (size_t)S_ * FF * 2;
constexpr size_t WS_TOTAL = OFF_O + (size_t)S_ * NH * DH * 2;

__global__ __launch_bounds__(256) void k_ln(const u16* __restrict__ x, u16* __restrict__ xn_ft) {
    const int row = blockIdx.x * 4 + (threadIdx.x >> 6), lane = threadIdx.x & 63;
    const v4u raw = *(const v4u*)(x + (size_t)row * DM + lane * 8);
    float v[8];
    v[0] = __uint_as_float(raw.x << 16); v[1] = __uint_as_float(raw.x & 0xffff0000u);
    v[2] = __uint_as_float(raw.y << 16); v[3] = __uint_as_float(raw.y & 0xffff0000u);
    v[4] = __uint_as_float(raw.z << 16); v[5] = __uint_as_float(raw.z & 0xffff0000u);
    v[6] = __uint_as_float(raw.w << 16); v[7] = __uint_as_float(raw.w & 0xffff0000u);
    float s = 0.f;
#pragma unroll
    for (int j = 0; j < 8; ++j) s += v[j];
#pragma unroll
    for (int o = 1; o < 64; o <<= 1) s += __shfl_xor(s, o);
    const float mean = s * (1.0f / DM);
    float q = 0.f;
#pragma unroll
    for (int j = 0; j < 8; ++j) { const float d = v[j] - mean; q += d * d; }
#pragma unroll
    for (int o = 1; o < 64; o <<= 1) q += __shfl_xor(q, o);
    const float mb = rbf(mean), vb = rbf(q * (1.0f / DM));
    const float den = rbf(sqrtf(rbf(vb + 1e-05f)));
    float y[8];
#pragma unroll
    for (int j = 0; j < 8; ++j) y[j] = rbf(v[j] - mb) / den;
    v4u o4 = {pack2(y[0], y[1]), pack2(y[2], y[3]), pack2(y[4], y[5]), pack2(y[6], y[7])};
    const int mt = row >> 4, kk = lane >> 2, fl = (lane & 3) * 16 + (row & 15);
    *(v4u*)(xn_ft + (((size_t)mt * 16 + kk) * 64 + fl) * 8) = o4;
}

__global__ __launch_bounds__(256) void k_tile_w(const u16* __restrict__ Wq, const u16* __restrict__ Wk, const u16* __restrict__ Wv,
                                                const u16* __restrict__ Wo, const u16* __restrict__ Wg, const u16* __restrict__ Wu,
                                                const u16* __restrict__ Wd, char* __restrict__ ws) {
    constexpr int C_WT = NH * 4 * 96 * 64, C_WOT = NH * 32 * 4 * 64, C_WG = 112 * 16 * 64, C_WD = 32 * 56 * 64;
    int c = blockIdx.x * 256 + threadIdx.x;
    const u16* src; size_t pitch; u16* dst;
    if (c < C_WT) {
        const int lane = c & 63, f = (c >> 6) % 96, hw = (c >> 6) / 96, h = hw >> 2, w = hw & 3;
        const int m = f >> 5, tt = (f >> 4) & 1, kk = f & 15, g = lane >> 4, i = lane & 15;
        const u16* W = m == 0 ? Wq : (m == 1 ? Wk : Wv);
        src = W + (size_t)(32 * kk + 8 * g) * (NH * DH) + h * DH + pi_col(2 * w + tt, i);
        pitch = NH * DH; dst = (u16*)(ws + OFF_WT) + (size_t)c * 8;
    } else if ((c -= C_WT) < C_WOT) {
        const int lane = c & 63, ks = (c >> 6) & 3, T = (c >> 8) & 31, h = c >> 13, g = lane >> 4, i = lane & 15;
        src = Wo + (size_t)(h * DH + 32 * ks + 8 * g) * DM + 16 * T + i;
        pitch = DM; dst = (u16*)(ws + OFF_WOT) + (size_t)c * 8;
    } else if ((c -= C_WOT) < 2 * C_WG) {
        const int which = c >= C_WG; if (which) c -= C_WG;
        const int lane = c & 63, kk = (c >> 6) & 15, nt = c >> 10, g = lane >> 4, i = lane & 15;
        src = (which ? Wu : Wg) + (size_t)(32 * kk + 8 * g) * FF + pi_col(nt, i);
        pitch = FF; dst = (u16*)(ws + (which ? OFF_WU : OFF_WG)) + (size_t)c * 8;
    } else if ((c -= 2 * C_WG) < C_WD) {
        const int lane = c & 63, kk = (c >> 6) % 56, nt = (c >> 6) / 56, g = lane >> 4, i = lane & 15;
        src = Wd + (size_t)(32 * kk + 8 * g) * DM + 16 * nt + i;
        pitch = DM; dst = (u16*)(ws + OFF_WD) + (size_t)c * 8;
    } else return;
    unsigned e[8];
#pragma unroll
    for (int j = 0; j < 8; ++j) e[j] = src[(size_t)j * pitch];
    v4u o4 = {e[0] | (e[1] << 16), e[2] | (e[3] << 16), e[4] | (e[5] << 16), e[6] | (e[7] << 16)};
    *(v4u*)dst = o4;
}
constexpr int TILE_CHUNKS = NH * 4 * 96 * 64 + NH * 32 * 4 * 64 + 2 * 112 * 16 * 64 + 32 * 56 * 64;

__global__ __launch_bounds__(256) void k_qkv(const bf16x8* __restrict__ XN, const bf16x8* __restrict__ WT,
                                             u16* __restrict__ QX, u16* __restrict__ KX, u16* __restrict__ VXT) {
    const int wid = blockIdx.x * 4 + (threadIdx.x >> 6), lane = threadIdx.x & 63, g = lane >> 4, i = lane & 15;
    const int half = wid & 1, h = (wid >> 1) & 31, rest = wid >> 6, m = rest % 3, mb = rest / 3;
    f32x4 acc[4][4];
#pragma unroll
    for (int a = 0; a < 4; ++a)
#pragma unroll
        for (int b = 0; b < 4; ++b) acc[a][b] = (f32x4){0.f, 0.f, 0.f, 0.f};
    const bf16x8* xa = XN + (size_t)(mb * 4) * 16 * 64 + lane;
    const bf16x8* wb = WT + ((size_t)(h * 4 + 2 * half) * 96 + m * 32) * 64 + lane;
    for (int kk = 0; kk < 16; ++kk) {
        bf16x8 a[4], b[4];
#pragma unroll
        for (int t = 0; t < 4; ++t) a[t] = xa[(t * 16 + kk) * 64];
#pragma unroll
        for (int t = 0; t < 4; ++t) b[t] = wb[((t >> 1) * 96 + (t & 1) * 16 + kk) * 64];
        if (m < 2) {
#pragma unroll
            for (int mt = 0; mt < 4; ++mt)
#pragma unroll
                for (int t = 0; t < 4; ++t) acc[mt][t] = MFMA(b[t], a[mt], acc[mt][t]);
        } else {
#pragma unroll
            for (int mt = 0; mt < 4; ++mt)
#pragma unroll
                for (int t = 0; t < 4; ++t) acc[mt][t] = MFMA(a[mt], b[t], acc[mt][t]);
        }
    }
    if (m < 2) {
        u16* dst = m == 0 ? QX : KX;
#pragma unroll
        for (int mt = 0; mt < 4; ++mt) {
            const int tok = mb * 64 + mt * 16 + i, s = tok >> 5, tl = tok & 31;
#pragma unroll
            for (int p = 0; p < 2; ++p) {
                const int ks = 2 * half + p;
                v4u o = {pack2(acc[mt][2 * p][0], acc[mt][2 * p][1]), pack2(acc[mt][2 * p][2], acc[mt][2 * p][3]),
                         pack2(acc[mt][2 * p + 1][0], acc[mt][2 * p + 1][1]), pack2(acc[mt][2 * p + 1][2], acc[mt][2 * p + 1][3])};
                *(v4u*)(dst + (((size_t)s * NH + h) * 32 + tl) * DH + 32 * ks + 8 * g) = o;
            }
        }
    } else {
#pragma unroll
        for (int mt = 0; mt < 4; ++mt) {
            const int tok = mb * 64 + mt * 16 + 4 * g, s = tok >> 5, tl = tok & 31;
#pragma unroll
            for (int t = 0; t < 4; ++t) {
                const int e = pi_col(4 * half + t, i);
                v2u o = {pack2(acc[mt][t][0], acc[mt][t][1]), pack2(acc[mt][t][2], acc[mt][t][3])};
                *(v2u*)(VXT + (((size_t)s * NH + h) * DH + e) * 32 + tl) = o;
            }
        }
    }
}

__global__ __launch_bounds__(256) void k_glu(const bf16x8* __restrict__ XN, const bf16x8* __restrict__ WG, const bf16x8* __restrict__ WU,
                                             bf16x8* __restrict__ Hft) {
    const int wid = blockIdx.x * 4 + (threadIdx.x >> 6), lane = threadIdx.x & 63;
    const int fp = wid % 56, mb = wid / 56;
    f32x4 ag[4][2], au[4][2];
#pragma unroll
    for (int a = 0; a < 4; ++a)
#pragma unroll
        for (int b = 0; b < 2; ++b) { ag[a][b] = (f32x4){0.f, 0.f, 0.f, 0.f}; au[a][b] = (f32x4){0.f, 0.f, 0.f, 0.f}; }
    const bf16x8* xa = XN + (size_t)(mb * 4) * 16 * 64 + lane;
    const bf16x8* wg = WG + (size_t)(fp * 2) * 16 * 64 + lane;
    const bf16x8* wu = WU + (size_t)(fp * 2) * 16 * 64 + lane;
    for (int kk = 0; kk < 16; ++kk) {
        bf16x8 a[4], bg[2], bu[2];
#pragma unroll
        for (int t = 0; t < 4; ++t) a[t] = xa[(t * 16 + kk) * 64];
#pragma unroll
        for (int t = 0; t < 2; ++t) { bg[t] = wg[(t * 16 + kk) * 64]; bu[t] = wu[(t * 16 + kk) * 64]; }
#pragma unroll
        for (int mt = 0; mt < 4; ++mt)
#pragma unroll
            for (int t = 0; t < 2; ++t) { ag[mt][t] = MFMA(bg[t], a[mt], ag[mt][t]); au[mt][t] = MFMA(bu[t], a[mt], au[mt][t]); }
    }
#pragma unroll
    for (int mt = 0; mt < 4; ++mt) {
        float hv[8];
#pragma unroll
        for (int t = 0; t < 2; ++t)
#pragma unroll
            for (int r = 0; r < 4; ++r) {
                const float gb = rbf(ag[mt][t][r]), ub = rbf(au[mt][t][r]);
                const float sg = rbf(1.0f / (1.0f + __expf(-gb)));
                hv[t * 4 + r] = sg * ub;
            }
        Hft[((size_t)(mb * 4 + mt) * 56 + fp) * 64 + lane] = mk8(pack2(hv[0], hv[1]), pack2(hv[2], hv[3]), pack2(hv[4], hv[5]), pack2(hv[6], hv[7]));
    }
}

constexpr int L_WO = 0, L_MEM = 131072, L_QE = L_MEM + 8192, L_KE = L_QE + 2048, L_VE = L_KE + 2048, L_KX = L_VE + 2048, L_VX = L_KX + 8192,
              L_TOTAL = L_VX + 8192;
constexpr unsigned SPINCAP = 1u << 20;
constexpr float SCALE = 0.08838834764831845f;

__global__ __launch_bounds__(256, 1) void k_chain(const u16* __restrict__ mem0, const bf16x8* __restrict__ WT, const bf16x8* __restrict__ WOT,
                                                  const u16* __restrict__ KX, const u16* __restrict__ VXT, u16* __restrict__ KM,
                                                  u16* __restrict__ VM, unsigned* Pbuf, unsigned* Mbuf, unsigned* tmo) {
    extern __shared__ __attribute__((aligned(16))) char lds[];
    const int h = blockIdx.x;
    const int w = __builtin_amdgcn_readfirstlane(threadIdx.x >> 6), lane = threadIdx.x & 63, g = lane >> 4, i = lane & 15;
    const __amdgpu_buffer_rsrc_t prs = __builtin_amdgcn_make_buffer_rsrc(Pbuf, 0, 32 * 16384, 0x00020000);
    const __amdgpu_buffer_rsrc_t mrs = __builtin_amdgcn_make_buffer_rsrc(Mbuf, 0, 64 * 8192, 0x00020000);

    bf16x8 W[96];
    {
        const bf16x8* src = WT + (size_t)(h * 4 + w) * 96 * 64 + lane;
#pragma unroll
        for (int f = 0; f < 96; ++f) W[f] = src[f * 64];
        const bf16x8* so = WOT + ((size_t)h * 128 + w * 32) * 64 + lane;
        bf16x8* lo = (bf16x8*)(lds + L_WO) + (w * 32) * 64 + lane;
#pragma unroll 8
        for (int f = 0; f < 32; ++f) lo[f * 64] = so[f * 64];
    }
    {
        const int tid = threadIdx.x;
#pragma unroll
        for (int c = tid; c < 512; c += 256) {
            const int t = c & 7, gg = (c >> 3) & 3, kk = c >> 5;
            *(v4u*)(lds + L_MEM + c * 16) = *(const v4u*)(mem0 + t * DM + 32 * kk + 8 * gg);
        }
    }
    v4u pk[2], pv[2];
    {
        const char* kb = (const char*)(KX + ((size_t)0 * NH + h) * 4096) + w * 2048 + lane * 16;
        const char* vb = (const char*)(VXT + ((size_t)0 * NH + h) * 4096) + w * 2048 + lane * 16;
        pk[0] = *(const v4u*)kb; pk[1] = *(const v4u*)(kb + 1024);
        pv[0] = *(const v4u*)vb; pv[1] = *(const v4u*)(vb + 1024);
    }
    const int red = h * 4 + w;

    for (int s = 0; s < NSEG; ++s) {
        __syncthreads();
        *(v4u*)(lds + L_KX + w * 2048 + lane * 16) = pk[0]; *(v4u*)(lds + L_KX + w * 2048 + 1024 + lane * 16) = pk[1];
        *(v4u*)(lds + L_VX + w * 2048 + lane * 16) = pv[0]; *(v4u*)(lds + L_VX + w * 2048 + 1024 + lane * 16) = pv[1];
        f32x4 acc[6];
#pragma unroll
        for (int a = 0; a < 6; ++a) acc[a] = (f32x4){0.f, 0.f, 0.f, 0.f};
        {
            const char* mp = lds + L_MEM + g * 128 + (i & 7) * 16;
#pragma unroll
            for (int kk = 0; kk < 16; ++kk) {
                const bf16x8 a = *(const bf16x8*)(mp + kk * 512);
                acc[0] = MFMA(W[kk], a, acc[0]);
                acc[1] = MFMA(W[16 + kk], a, acc[1]);
                acc[2] = MFMA(W[32 + kk], a, acc[2]);
                acc[3] = MFMA(W[48 + kk], a, acc[3]);
                acc[4] = MFMA(a, W[64 + kk], acc[4]);
                acc[5] = MFMA(a, W[80 + kk], acc[5]);
            }
        }
        {
            const v4u qf = {pack2(acc[0][0], acc[0][1]), pack2(acc[0][2], acc[0][3]), pack2(acc[1][0], acc[1][1]), pack2(acc[1][2], acc[1][3])};
            const v4u kf = {pack2(acc[2][0], acc[2][1]), pack2(acc[2][2], acc[2][3]), pack2(acc[3][0], acc[3][1]), pack2(acc[3][2], acc[3][3])};
            const v2u v0 = {pack2(acc[4][0], acc[4][1]), pack2(acc[4][2], acc[4][3])};
            const v2u v1 = {pack2(acc[5][0], acc[5][1]), pack2(acc[5][2], acc[5][3])};
            if (i < 8) {
                *(v4u*)(lds + L_QE + ((w * 4 + g) * 8 + i) * 16) = qf;
                *(v4u*)(lds + L_KE + ((w * 4 + g) * 8 + i) * 16) = kf;
                *(v4u*)(KM + (((size_t)s * NH + h) * MT + i) * DH + 32 * w + 8 * g) = kf;
            }
            if (g < 2) {
                *(v2u*)(lds + L_VE + ((2 * w) * 32 + lane) * 8) = v0;
                *(v2u*)(lds + L_VE + ((2 * w + 1) * 32 + lane) * 8) = v1;
                *(v2u*)(VM + ((((size_t)s * NH + h) * 8 + 2 * w) * 32 + lane) * 4) = v0;
                *(v2u*)(VM + ((((size_t)s * NH + h) * 8 + 2 * w + 1) * 32 + lane) * 4) = v1;
            }
        }
        if (s == NSEG - 1) break;
        __syncthreads();
        bf16x8 of[4];
        {
            f32x4 sc[3];
#pragma unroll
            for (int a = 0; a < 3; ++a) sc[a] = (f32x4){0.f, 0.f, 0.f, 0.f};
            const int key1 = 8 * (i >> 2) + (i & 3);
#pragma unroll
            for (int ks = 0; ks < 4; ++ks) {
                const bf16x8 qf = *(const bf16x8*)(lds + L_QE + ((ks * 4 + g) * 8 + (i & 7)) * 16);
                const bf16x8 kf = *(const bf16x8*)(lds + L_KE + ((ks * 4 + g) * 8 + (i & 7)) * 16);
                const bf16x8 k1 = *(const bf16x8*)(lds + L_KX + key1 * 256 + ks * 64 + g * 16);
                const bf16x8 k2 = *(const bf16x8*)(lds + L_KX + (key1 + 4) * 256 + ks * 64 + g * 16);
                sc[0] = MFMA(kf, qf, sc[0]);
                sc[1] = MFMA(k1, qf, sc[1]);
                sc[2] = MFMA(k2, qf, sc[2]);
            }
            float sv[12];
#pragma unroll
            for (int a = 0; a < 3; ++a)
#pragma unroll
                for (int r = 0; r < 4; ++r) sv[a * 4 + r] = rbf(rbf(sc[a][r]) * SCALE);
            const bool mv = g < 2;
            float mx = fmaxf(fmaxf(fmaxf(sv[4], sv[5]), fmaxf(sv[6], sv[7])), fmaxf(fmaxf(sv[8], sv[9]), fmaxf(sv[10], sv[11])));
            const float mxm = fmaxf(fmaxf(sv[0], sv[1]), fmaxf(sv[2], sv[3]));
            mx = mv ? fmaxf(mx, mxm) : mx;
            mx = fmaxf(mx, __shfl_xor(mx, 16));
            mx = fmaxf(mx, __shfl_xor(mx, 32));
            float ex[12], sum = 0.f;
#pragma unroll
            for (int a = 0; a < 12; ++a) { ex[a] = __expf(sv[a] - mx); if (a < 4) ex[a] = mv ? ex[a] : 0.f; sum += ex[a]; }
            sum += __shfl_xor(sum, 16);
            sum += __shfl_xor(sum, 32);
            const float inv = 1.0f / sum;
            const bf16x8 pX = mk8(pack2(ex[4] * inv, ex[5] * inv), pack2(ex[6] * inv, ex[7] * inv), pack2(ex[8] * inv, ex[9] * inv), pack2(ex[10] * inv, ex[11] * inv));
            const bf16x8 pM = mk8(pack2(ex[0] * inv, ex[1] * inv), pack2(ex[2] * inv, ex[3] * inv), 0u, 0u);
#pragma unroll
            for (int ks = 0; ks < 4; ++ks) {
                f32x4 o[2];
#pragma unroll
                for (int t = 0; t < 2; ++t) {
                    const int tau = 2 * ks + t;
                    const bf16x8 vx = *(const bf16x8*)(lds + L_VX + pi_col(tau, i) * 64 + g * 16);
                    const v2u vm = *(const v2u*)(lds + L_VE + (tau * 32 + (lane & 31)) * 8);
                    o[t] = MFMA(vx, pX, ((f32x4){0.f, 0.f, 0.f, 0.f}));
                    o[t] = MFMA(mk8(vm.x, vm.y, 0u, 0u), pM, o[t]);
                }
                of[ks] = mk8(pack2(o[0][0], o[0][1]), pack2(o[0][2], o[0][3]), pack2(o[1][0], o[1][1]), pack2(o[1][2], o[1][3]));
            }
        }
        const unsigned tag = 1u + (unsigned)(s % 3);
        {
            const char* wp = lds + L_WO + (w * 32) * 1024 + lane * 16;
#pragma unroll
            for (int a = 0; a < 8; a += 2) {
                f32x4 d0 = {0.f, 0.f, 0.f, 0.f}, d1 = {0.f, 0.f, 0.f, 0.f};
#pragma unroll
                for (int ks = 0; ks < 4; ++ks) {
                    d0 = MFMA(*(const bf16x8*)(wp + (a * 4 + ks) * 1024), of[ks], d0);
                    d1 = MFMA(*(const bf16x8*)(wp + (a * 4 + 4 + ks) * 1024), of[ks], d1);
                }
                v4u v;
#pragma unroll
                for (int r = 0; r < 4; ++r) {
                    const unsigned x0 = __float_as_uint(d0[r]);
                    const unsigned x1 = (unsigned)__builtin_amdgcn_update_dpp(0, (int)__float_as_uint(d1[r]), 0x128, 0xf, 0xf, false);
                    v[r] = ((i < 8 ? x0 : x1) & ~3u) | tag;
                }
                const int T = 8 * w + a + (i >> 3);
                __builtin_amdgcn_raw_buffer_store_b128(v, prs, (unsigned)(h * 16384 + (4 * T + g) * 128 + (i & 7) * 16), 0, 16);
            }
        }
        {
            const char* kb = (const char*)(KX + ((size_t)(s + 1) * NH + h) * 4096) + w * 2048 + lane * 16;
            const char* vb = (const char*)(VXT + ((size_t)(s + 1) * NH + h) * 4096) + w * 2048 + lane * 16;
            pk[0] = *(const v4u*)kb; pk[1] = *(const v4u*)(kb + 1024);
            pv[0] = *(const v4u*)vb; pv[1] = *(const v4u*)(vb + 1024);
        }
        for (int z = 0; z < 12; ++z) __builtin_amdgcn_s_sleep(1);
        {
            v4u r[4];
            unsigned spins = 0;
            for (;;) {
#pragma unroll
                for (int q = 0; q < 4; ++q)
                    r[q] = __builtin_amdgcn_raw_buffer_load_b128(prs, (unsigned)((q * 8 + (lane >> 3)) * 16384 + red * 128 + (lane & 7) * 16), 0, 16);
                bool ok = true;
#pragma unroll
                for (int q = 0; q < 4; ++q) ok &= ((r[q].x & 3u) == tag) & ((r[q].y & 3u) == tag) & ((r[q].z & 3u) == tag) & ((r[q].w & 3u) == tag);
                if (__all(ok)) break;
                if (++spins > SPINCAP) { if (lane == 0) atomicAdd(tmo, 1u); break; }
            }
            float sm[4];
#pragma unroll
            for (int c = 0; c < 4; ++c) {
                sm[c] = (__uint_as_float(r[0][c] & ~3u) + __uint_as_float(r[1][c] & ~3u)) + (__uint_as_float(r[2][c] & ~3u) + __uint_as_float(r[3][c] & ~3u));
                sm[c] += __shfl_xor(sm[c], 8);
                sm[c] += __shfl_xor(sm[c], 16);
                sm[c] += __shfl_xor(sm[c], 32);
            }
            if (lane < 8) {
                v2u v = {pack2(sm[0], sm[1]), pack2(sm[2], sm[3])};
                if (v.x == 0u) v.x = 0x8000u;
                if (v.y == 0u) v.y = 0x8000u;
                __builtin_amdgcn_raw_buffer_store_b64(v, mrs, (unsigned)(s * 8192 + red * 64 + lane * 8), 0, 16);
            }
        }
        for (int z = 0; z < 12; ++z) __builtin_amdgcn_s_sleep(1);
        {
            v4u m[2];
            unsigned spins = 0;
            for (;;) {
#pragma unroll
                for (int q = 0; q < 2; ++q) m[q] = __builtin_amdgcn_raw_buffer_load_b128(mrs, (unsigned)(s * 8192 + w * 2048 + q * 1024 + lane * 16), 0, 16);
                bool ok = true;
#pragma unroll
                for (int q = 0; q < 2; ++q) ok &= (m[q].x != 0u) & (m[q].y != 0u) & (m[q].z != 0u) & (m[q].w != 0u);
                if (__all(ok)) break;
                if (++spins > SPINCAP) { if (lane == 0) atomicAdd(tmo, 1u); break; }
            }
#pragma unroll
            for (int q = 0; q < 2; ++q) {
                const int R = w * 32 + q * 16 + (lane >> 2), t = 2 * (lane & 3);
                char* dst = lds + L_MEM + (R >> 3) * 512 + ((R >> 1) & 3) * 128 + t * 16 + (R & 1) * 8;
                *(v2u*)dst = (v2u){m[q].x, m[q].y};
                *(v2u*)(dst + 16) = (v2u){m[q].z, m[q].w};
            }
        }
    }
}

__global__ __launch_bounds__(256) void k_xattn(const u16* __restrict__ QX, const u16* __restrict__ KX, const u16* __restrict__ VXT,
                                               const u16* __restrict__ KM, const u16* __restrict__ VM, bf16x8* __restrict__ Oft) {
    const int wid = blockIdx.x * 4 + (threadIdx.x >> 6), lane = threadIdx.x & 63, g = lane >> 4, i = lane & 15;
    const int s = wid >> 5, h = wid & 31;
    const size_t sh = (size_t)s * NH + h;
    const u16* qb = QX + sh * 4096; const u16* kb = KX + sh * 4096; const u16* vb = VXT + sh * 4096;
    const u16* kmb = KM + sh * 1024; const u16* vmb = VM + sh * 1024;
    const int key1 = 8 * (i >> 2) + (i & 3);
    bf16x8 kf[4], k1[4], k2[4];
#pragma unroll
    for (int ks = 0; ks < 4; ++ks) {
        kf[ks] = *(const bf16x8*)(kmb + (i & 7) * DH + 32 * ks + 8 * g);
        k1[ks] = *(const bf16x8*)(kb + key1 * DH + 32 * ks + 8 * g);
        k2[ks] = *(const bf16x8*)(kb + (key1 + 4) * DH + 32 * ks + 8 * g);
    }
#pragma unroll
    for (int qt = 0; qt < 2; ++qt) {
        f32x4 sc[3];
#pragma unroll
        for (int a = 0; a < 3; ++a) sc[a] = (f32x4){0.f, 0.f, 0.f, 0.f};
#pragma unroll
        for (int ks = 0; ks < 4; ++ks) {
            const bf16x8 qf = *(const bf16x8*)(qb + (qt * 16 + i) * DH + 32 * ks + 8 * g);
            sc[0] = MFMA(kf[ks], qf, sc[0]);
            sc[1] = MFMA(k1[ks], qf, sc[1]);
            sc[2] = MFMA(k2[ks], qf, sc[2]);
        }
        const int tq = qt * 16 + i;
        float sv[12]; bool ok[12];
#pragma unroll
        for (int r = 0; r < 4; ++r) { ok[r] = g < 2; ok[4 + r] = (8 * g + r) <= tq; ok[8 + r] = (8 * g + 4 + r) <= tq; }
        float mx = -3.0e38f;
#pragma unroll
        for (int a = 0; a < 3; ++a)
#pragma unroll
            for (int r = 0; r < 4; ++r) { sv[a * 4 + r] = rbf(rbf(sc[a][r]) * SCALE); mx = ok[a * 4 + r] ? fmaxf(mx, sv[a * 4 + r]) : mx; }
        mx = fmaxf(mx, __shfl_xor(mx, 16));
        mx = fmaxf(mx, __shfl_xor(mx, 32));
        float ex[12], sum = 0.f;
#pragma unroll
        for (int a = 0; a < 12; ++a) { ex[a] = ok[a] ? __expf(sv[a] - mx) : 0.f; sum += ex[a]; }
        sum += __shfl_xor(sum, 16);
        sum += __shfl_xor(sum, 32);
        const float inv = 1.0f / sum;
        const bf16x8 pX = mk8(pack2(ex[4] * inv, ex[5] * inv), pack2(ex[6] * inv, ex[7] * inv), pack2(ex[8] * inv, ex[9] * inv), pack2(ex[10] * inv, ex[11] * inv));
        const bf16x8 pM = mk8(pack2(ex[0] * inv, ex[1] * inv), pack2(ex[2] * inv, ex[3] * inv), 0u, 0u);
#pragma unroll
        for (int ks = 0; ks < 4; ++ks) {
            f32x4 o[2];
#pragma unroll
            for (int t = 0; t < 2; ++t) {
                const int tau = 2 * ks + t;
                const bf16x8 vx = *(const bf16x8*)(vb + pi_col(tau, i) * 32 + g * 8);
                const v2u vm = *(const v2u*)(vmb + (tau * 32 + (lane & 31)) * 4);
                o[t] = MFMA(vx, pX, ((f32x4){0.f, 0.f, 0.f, 0.f}));
                o[t] = MFMA(mk8(vm.x, vm.y, 0u, 0u), pM, o[t]);
            }
            Oft[((size_t)(s * 2 + qt) * 128 + h * 4 + ks) * 64 + lane] =
                mk8(pack2(o[0][0], o[0][1]), pack2(o[0][2], o[0][3]), pack2(o[1][0], o[1][1]), pack2(o[1][2], o[1][3]));
        }
    }
}

__global__ __launch_bounds__(256) void k_final(const bf16x8* __restrict__ Oft, const bf16x8* __restrict__ WOT, const bf16x8* __restrict__ Hft,
                                               const bf16x8* __restrict__ WD, const u16* __restrict__ x, u16* __restrict__ out) {
    const int wid = blockIdx.x * 4 + (threadIdx.x >> 6), lane = threadIdx.x & 63, g = lane >> 4, i = lane & 15;
    const int nb = wid & 15, mb = wid >> 4;
    f32x4 aa[2][2], ab[2][2];
#pragma unroll
    for (int a = 0; a < 2; ++a)
#pragma unroll
        for (int b = 0; b < 2; ++b) { aa[a][b] = (f32x4){0.f, 0.f, 0.f, 0.f}; ab[a][b] = (f32x4){0.f, 0.f, 0.f, 0.f}; }
    {
        const bf16x8* pa = Oft + (size_t)(mb * 2) * 128 * 64 + lane;
#pragma unroll 4
        for (int kk = 0; kk < 128; ++kk) {
            const bf16x8 a0 = pa[kk * 64], a1 = pa[(128 + kk) * 64];
            const bf16x8 b0 = WOT[(((size_t)(kk >> 2) * 32 + nb * 2) * 4 + (kk & 3)) * 64 + lane];
            const bf16x8 b1 = WOT[(((size_t)(kk >> 2) * 32 + nb * 2 + 1) * 4 + (kk & 3)) * 64 + lane];
            aa[0][0] = MFMA(b0, a0, aa[0][0]); aa[0][1] = MFMA(b1, a0, aa[0][1]);
            aa[1][0] = MFMA(b0, a1, aa[1][0]); aa[1][1] = MFMA(b1, a1, aa[1][1]);
        }
    }
    {
        const bf16x8* pa = Hft + (size_t)(mb * 2) * 56 * 64 + lane;
        const bf16x8* pb = WD + (size_t)(nb * 2) * 56 * 64 + lane;
#pragma unroll 4
        for (int kk = 0; kk < 56; ++kk) {
            const bf16x8 a0 = pa[kk * 64], a1 = pa[(56 + kk) * 64];
            const bf16x8 b0 = pb[kk * 64], b1 = pb[(56 + kk) * 64];
            ab[0][0] = MFMA(b0, a0, ab[0][0]); ab[0][1] = MFMA(b1, a0, ab[0][1]);
            ab[1][0] = MFMA(b0, a1, ab[1][0]); ab[1][1] = MFMA(b1, a1, ab[1][1]);
        }
    }
#pragma unroll
    for (int mt = 0; mt < 2; ++mt)
#pragma unroll
        for (int t = 0; t < 2; ++t) {
            const size_t idx = (size_t)(mb * 32 + mt * 16 + i) * DM + (nb * 2 + t) * 16 + 4 * g;
            const v2u xr = *(const v2u*)(x + idx);
            float xv[4] = {__uint_as_float(xr.x << 16), __uint_as_float(xr.x & 0xffff0000u), __uint_as_float(xr.y << 16), __uint_as_float(xr.y & 0xffff0000u)};
            float y[4];
#pragma unroll
            for (int r = 0; r < 4; ++r) y[r] = rbf(xv[r] + rbf(aa[mt][t][r])) + rbf(ab[mt][t][r]);
            *(v2u*)(out + idx) = (v2u){pack2(y[0], y[1]), pack2(y[2], y[3])};
        }
}

extern "C" void kernel_launch(void* const* d_in, const int* in_sizes, int n_in, void* d_out, int out_size, void* d_ws, size_t ws_size,
                              hipStream_t stream) {
    const u16* x = (const u16*)d_in[0];
    const u16* mem0 = (const u16*)d_in[1];
    const u16* Wq = (const u16*)d_in[2];
    const u16* Wk = (const u16*)d_in[3];
    const u16* Wv = (const u16*)d_in[4];
    const u16* Wo = (const u16*)d_in[5];
    const u16* Wg = (const u16*)d_in[8];
    const u16* Wu = (const u16*)d_in[9];
    const u16* Wd = (const u16*)d_in[10];
    char* ws = (char*)d_ws;
    (void)hipFuncSetAttribute((const void*)k_chain, hipFuncAttributeMaxDynamicSharedMemorySize, L_TOTAL);
    (void)hipMemsetAsync(ws, 0, ZERO_BYTES, stream);
    k_ln<<<S_ / 4, 256, 0, stream>>>(x, (u16*)(ws + OFF_XN));
    k_tile_w<<<(TILE_CHUNKS + 255) / 256, 256, 0, stream>>>(Wq, Wk, Wv, Wo, Wg, Wu, Wd, ws);
    k_qkv<<<32 * 3 * 64 / 4, 256, 0, stream>>>((const bf16x8*)(ws + OFF_XN), (const bf16x8*)(ws + OFF_WT), (u16*)(ws + OFF_QX), (u16*)(ws + OFF_KX),
                                               (u16*)(ws + OFF_VXT));
    k_glu<<<32 * 56 / 4, 256, 0, stream>>>((const bf16x8*)(ws + OFF_XN), (const bf16x8*)(ws + OFF_WG), (const bf16x8*)(ws + OFF_WU),
                                           (bf16x8*)(ws + OFF_H));
    k_chain<<<NH, 256, L_TOTAL, stream>>>(mem0, (const bf16x8*)(ws + OFF_WT), (const bf16x8*)(ws + OFF_WOT), (const u16*)(ws + OFF_KX),
                                          (const u16*)(ws + OFF_VXT), (u16*)(ws + OFF_KM), (u16*)(ws + OFF_VM), (unsigned*)(ws + OFF_P),
                                          (unsigned*)(ws + OFF_M), (unsigned*)(ws + OFF_TMO));
    k_xattn<<<NSEG * NH / 4, 256, 0, stream>>>((const u16*)(ws + OFF_QX), (const u16*)(ws + OFF_KX), (const u16*)(ws + OFF_VXT),
                                               (const u16*)(ws + OFF_KM), (const u16*)(ws + OFF_VM), (bf16x8*)(ws + OFF_O));
    k_final<<<64 * 16 / 4, 256, 0, stream>>>((const bf16x8*)(ws + OFF_O), (const bf16x8*)(ws + OFF_WOT), (const bf16x8*)(ws + OFF_H),
                                             (const bf16x8*)(ws + OFF_WD), x, (u16*)d_out);
}
```

```cpp
#include <hip/hip_runtime.h>
#include <hip/hip_bf16.h>

constexpr int S_ = 2048, DM = 512, NH = 32, DH = 128, MT = 8, SEG = 32, NSEG = 64, FF = 1792;

typedef unsigned short u16;
typedef __attribute__((ext_vector_type(8))) short bf16x8;
typedef __attribute__((ext_vector_type(4))) float f32x4;
typedef __attribute__((ext_vector_type(4))) unsigned v4u;
typedef __attribute__((ext_vector_type(2))) unsigned v2u;
typedef __bf16 bf16x2_t __attribute__((ext_vector_type(2)));

__device__ __forceinline__ unsigned pack2(float a, float b) {
    bf16x2_t v = {(__bf16)a, (__bf16)b};
    return __builtin_bit_cast(unsigned, v);
}
__device__ __forceinline__ float rbf(float a) { return (float)(__bf16)a; }
__device__ __forceinline__ float bf2f(u16 b) { return __uint_as_float(((unsigned)b) << 16); }
__device__ __forceinline__ bf16x8 mk8(unsigned a, unsigned b, unsigned c, unsigned d) {
    v4u v = {a, b, c, d};
    return __builtin_bit_cast(bf16x8, v);
}
#define MFMA(a, b, c) __builtin_amdgcn_mfma_f32_16x16x32_bf16((a), (b), (c), 0, 0, 0)

__device__ __forceinline__ int pi_col(int tau, int rho) { return 32 * (tau >> 1) + 8 * (rho >> 2) + 4 * (tau & 1) + (rho & 3); }

constexpr size_t OFF_TMO = 0;
constexpr size_t OFF_M   = 256;
constexpr size_t OFF_P   = OFF_M + 64 * 8192;
constexpr size_t ZERO_BYTES = OFF_P + 32 * 16384;
constexpr size_t OFF_XN  = (ZERO_BYTES + 4095) / 4096 * 4096;
constexpr size_t OFF_WT  = OFF_XN + (size_t)S_ * DM * 2;
constexpr size_t OFF_WOT = OFF_WT + (size_t)3 * DM * NH * DH * 2;
constexpr size_t OFF_WG  = OFF_WOT + (size_t)DM * NH * DH * 2;
constexpr size_t OFF_WU  = OFF_WG + (size_t)DM * FF * 2;
constexpr size_t OFF_WD  = OFF_WU + (size_t)DM * FF * 2;
constexpr size_t OFF_QX  = OFF_WD + (size_t)DM * FF * 2;
constexpr size_t OFF_KX  = OFF_QX + (size_t)S_ * NH * DH * 2;
constexpr size_t OFF_VXT = OFF_KX + (size_t)S_ * NH * DH * 2;
constexpr size_t OFF_KM  = OFF_VXT + (size_t)S_ * NH * DH * 2;
constexpr size_t OFF_VM  = OFF_KM + (size_t)NSEG * NH * MT * DH * 2;
constexpr size_t OFF_H   = OFF_VM + (size_t)NSEG * NH * MT * DH * 2;
constexpr size_t OFF_O   = OFF_H + (size_t)S_ * FF * 2;
constexpr size_t WS_TOTAL = OFF_O + (size_t)S_ * NH * DH * 2;

__device__ __forceinline__ void ln_row(const u16* __restrict__ x, u16* __restrict__ xn_ft, int row, int lane) {
    const v4u raw = *(const v4u*)(x + (size_t)row * DM + lane * 8);
    float v[8];
    v[0] = __uint_as_float(raw.x << 16); v[1] = __uint_as_float(raw.x & 0xffff0000u);
    v[2] = __uint_as_float(raw.y << 16); v[3] = __uint_as_float(raw.y & 0xffff0000u);
    v[4] = __uint_as_float(raw.z << 16); v[5] = __uint_as_float(raw.z & 0xffff0000u);
    v[6] = __uint_as_float(raw.w << 16); v[7] = __uint_as_float(raw.w & 0xffff0000u);
    float s = 0.f;
#pragma unroll
    for (int j = 0; j < 8; ++j) s += v[j];
#pragma unroll
    for (int o = 1; o < 64; o <<= 1) s += __shfl_xor(s, o);
    const float mean = s * (1.0f / DM);
    float q = 0.f;
#pragma unroll
    for (int j = 0; j < 8; ++j) { const float d = v[j] - mean; q += d * d; }
#pragma unroll
    for (int o = 1; o < 64; o <<= 1) q += __shfl_xor(q, o);
    const float mb = rbf(mean), vb = rbf(q * (1.0f / DM));
    const float den = rbf(sqrtf(rbf(vb + 1e-05f)));
    float y[8];
#pragma unroll
    for (int j = 0; j < 8; ++j) y[j] = rbf(v[j] - mb) / den;
    v4u o4 = {pack2(y[0], y[1]), pack2(y[2], y[3]), pack2(y[4], y[5]), pack2(y[6], y[7])};
    const int mt = row >> 4, kk = lane >> 2, fl = (lane & 3) * 16 + (row & 15);
    *(v4u*)(xn_ft + (((size_t)mt * 16 + kk) * 64 + fl) * 8) = o4;
}

__device__ __forceinline__ void transpose8x8(const v4u (&r)[8], v4u (&o)[8]) {
#pragma unroll
    for (int c = 0; c < 4; ++c) {
#pragma unroll
        for (int d = 0; d < 4; ++d) {
            o[2 * c][d] = __builtin_amdgcn_perm(r[2 * d + 1][c], r[2 * d][c], 0x05040100u);
            o[2 * c + 1][d] = __builtin_amdgcn_perm(r[2 * d + 1][c], r[2 * d][c], 0x07060302u);
        }
    }
}
constexpr int B_WT = 3 * 64 * 512, B_WOT = 512 * 64, B_WG = 64 * 224, B_WD = 224 * 64;
constexpr int TILE_BLOCKS = B_WT + B_WOT + 2 * B_WG + B_WD;
__device__ __forceinline__ void tile_w_block(int c, const u16* __restrict__ Wq, const u16* __restrict__ Wk, const u16* __restrict__ Wv,
                                             const u16* __restrict__ Wo, const u16* __restrict__ Wg, const u16* __restrict__ Wu,
                                             const u16* __restrict__ Wd, char* __restrict__ ws) {
    const u16* src; int pitch; u16* d0; int dstep;
    bool perm;
    if (c < B_WT) {
        const int nb = c & 511, kb = (c >> 9) & 63, m = c >> 15;
        const int h = nb >> 4, ks = (nb >> 2) & 3, gp = nb & 3, kk = kb >> 2, g = kb & 3;
        src = (m == 0 ? Wq : (m == 1 ? Wk : Wv)) + (size_t)(8 * kb) * 4096 + 8 * nb; pitch = 4096;
        d0 = (u16*)(ws + OFF_WT) + ((size_t)((h * 4 + ks) * 96 + m * 32 + kk) * 64 + g * 16 + 4 * gp) * 8; dstep = 16 * 64 * 8; perm = true;
    } else if ((c -= B_WT) < B_WOT) {
        const int nb = c & 63, kb = c >> 6, h = kb >> 4, ks = (kb >> 2) & 3, g = kb & 3, T = nb >> 1;
        src = Wo + (size_t)(8 * kb) * 512 + 8 * nb; pitch = 512;
        d0 = (u16*)(ws + OFF_WOT) + ((size_t)((h * 32 + T) * 4 + ks) * 64 + g * 16 + 8 * (nb & 1)) * 8; dstep = 0; perm = false;
    } else if ((c -= B_WOT) < 2 * B_WG) {
        const int which = c >= B_WG; if (which) c -= B_WG;
        const int nb = c % 224, kb = c / 224, fp = nb >> 2, gp = nb & 3, kk = kb >> 2, g = kb & 3;
        src = (which ? Wu : Wg) + (size_t)(8 * kb) * FF + 8 * nb; pitch = FF;
        d0 = (u16*)(ws + (which ? OFF_WU : OFF_WG)) + ((size_t)((2 * fp) * 16 + kk) * 64 + g * 16 + 4 * gp) * 8; dstep = 16 * 64 * 8; perm = true;
    } else {
        c -= 2 * B_WG;
        const int nb = c & 63, kb = c >> 6, kk = kb >> 2, g = kb & 3, nt = nb >> 1;
        src = Wd + (size_t)(8 * kb) * 512 + 8 * nb; pitch = 512;
        d0 = (u16*)(ws + OFF_WD) + ((size_t)(nt * 56 + kk) * 64 + g * 16 + 8 * (nb & 1)) * 8; dstep = 0; perm = false;
    }
    v4u r[8], o[8];
#pragma unroll
    for (int j = 0; j < 8; ++j) r[j] = *(const v4u*)(src + (size_t)j * pitch);
    transpose8x8(r, o);
#pragma unroll
    for (int j = 0; j < 8; ++j) *(v4u*)(perm ? d0 + (j >> 2) * dstep + (j & 3) * 8 : d0 + j * 8) = o[j];
}

constexpr int PREP_LN_BLOCKS = S_ / 4;
__global__ __launch_bounds__(256) void k_prep(const u16* __restrict__ x, const u16* __restrict__ Wq, const u16* __restrict__ Wk,
                                              const u16* __restrict__ Wv, const u16* __restrict__ Wo, const u16* __restrict__ Wg,
                                              const u16* __restrict__ Wu, const u16* __restrict__ Wd, char* __restrict__ ws) {
    if (blockIdx.x < PREP_LN_BLOCKS) { ln_row(x, (u16*)(ws + OFF_XN), blockIdx.x * 4 + (threadIdx.x >> 6), threadIdx.x & 63); return; }
    const int c = (blockIdx.x - PREP_LN_BLOCKS) * 256 + threadIdx.x;
    if (c < TILE_BLOCKS) tile_w_block(c, Wq, Wk, Wv, Wo, Wg, Wu, Wd, ws);
}

__device__ __forceinline__ void qkv_task(const bf16x8* __restrict__ XN, const bf16x8* __restrict__ WT, u16* __restrict__ QX,
                                         u16* __restrict__ KX, u16* __restrict__ VXT, int mb, int m, int h, int half, int lane) {
    const int g = lane >> 4, i = lane & 15;
    f32x4 acc[4][4];
#pragma unroll
    for (int a = 0; a < 4; ++a)
#pragma unroll
        for (int b = 0; b < 4; ++b) acc[a][b] = (f32x4){0.f, 0.f, 0.f, 0.f};
    const bf16x8* xa = XN + (size_t)(mb * 4) * 16 * 64 + lane;
    const bf16x8* wb = WT + ((size_t)(h * 4 + 2 * half) * 96 + m * 32) * 64 + lane;
    for (int kk = 0; kk < 16; ++kk) {
        bf16x8 a[4], b[4];
#pragma unroll
        for (int t = 0; t < 4; ++t) a[t] = xa[(t * 16 + kk) * 64];
#pragma unroll
        for (int t = 0; t < 4; ++t) b[t] = wb[((t >> 1) * 96 + (t & 1) * 16 + kk) * 64];
        if (m < 2) {
#pragma unroll
            for (int mt = 0; mt < 4; ++mt)
#pragma unroll
                for (int t = 0; t < 4; ++t) acc[mt][t] = MFMA(b[t], a[mt], acc[mt][t]);
        } else {
#pragma unroll
            for (int mt = 0; mt < 4; ++mt)
#pragma unroll
                for (int t = 0; t < 4; ++t) acc[mt][t] = MFMA(a[mt], b[t], acc[mt][t]);
        }
    }
    if (m < 2) {
        u16* dst = m == 0 ? QX : KX;
#pragma unroll
        for (int mt = 0; mt < 4; ++mt) {
            const int tok = mb * 64 + mt * 16 + i, s = tok >> 5, tl = tok & 31;
            const int tile = m == 0 ? (tl >> 4) : ((tl >> 2) & 1), row = m == 0 ? (tl & 15) : (4 * (tl >> 3) + (tl & 3));
#pragma unroll
            for (int p = 0; p < 2; ++p) {
                const int ks = 2 * half + p;
                v4u o = {pack2(acc[mt][2 * p][0], acc[mt][2 * p][1]), pack2(acc[mt][2 * p][2], acc[mt][2 * p][3]),
                         pack2(acc[mt][2 * p + 1][0], acc[mt][2 * p + 1][1]), pack2(acc[mt][2 * p + 1][2], acc[mt][2 * p + 1][3])};
                *(v4u*)(dst + ((size_t)s * NH + h) * 4096 + ((tile * 4 + ks) * 64 + g * 16 + row) * 8) = o;
            }
        }
    } else {
#pragma unroll
        for (int mt = 0; mt < 4; ++mt) {
            const int s = mb * 2 + (mt >> 1), gp = 2 * (mt & 1) + (g >> 1);
#pragma unroll
            for (int t = 0; t < 4; ++t) {
                const int tau = 4 * half + t;
                v2u o = {pack2(acc[mt][t][0], acc[mt][t][1]), pack2(acc[mt][t][2], acc[mt][t][3])};
                *(v2u*)(VXT + ((size_t)s * NH + h) * 4096 + (tau * 64 + gp * 16 + i) * 8 + (g & 1) * 4) = o;
            }
        }
    }
}

__global__ __launch_bounds__(256) void k_kv(const bf16x8* __restrict__ XN, const bf16x8* __restrict__ WT, u16* __restrict__ KX,
                                            u16* __restrict__ VXT) {
    const int wid = blockIdx.x * 4 + (threadIdx.x >> 6);
    qkv_task(XN, WT, nullptr, KX, VXT, wid >> 7, 1 + ((wid >> 6) & 1), (wid >> 1) & 31, wid & 1, threadIdx.x & 63);
}

__device__ __forceinline__ void glu_block(const bf16x8* __restrict__ XN, const bf16x8* __restrict__ WG, const bf16x8* __restrict__ WU,
                                          const bf16x8* __restrict__ WD, u16* __restrict__ Bout, char* lds, int mb, int w, int lane) {
    const int g = lane >> 4, i = lane & 15;
    bf16x8* Hl = (bf16x8*)lds;
    const bf16x8* xa = XN + (size_t)(mb * 2) * 16 * 64 + lane;
    for (int fp = w; fp < 56; fp += 4) {
        f32x4 ag[2][2], au[2][2];
#pragma unroll
        for (int a = 0; a < 2; ++a)
#pragma unroll
            for (int b = 0; b < 2; ++b) { ag[a][b] = (f32x4){0.f, 0.f, 0.f, 0.f}; au[a][b] = (f32x4){0.f, 0.f, 0.f, 0.f}; }
        const bf16x8* wg = WG + (size_t)(fp * 2) * 16 * 64 + lane;
        const bf16x8* wu = WU + (size_t)(fp * 2) * 16 * 64 + lane;
#pragma unroll 4
        for (int kk = 0; kk < 16; ++kk) {
            bf16x8 a[2], bg[2], bu[2];
#pragma unroll
            for (int t = 0; t < 2; ++t) { a[t] = xa[(t * 16 + kk) * 64]; bg[t] = wg[(t * 16 + kk) * 64]; bu[t] = wu[(t * 16 + kk) * 64]; }
#pragma unroll
            for (int mt = 0; mt < 2; ++mt)
#pragma unroll
                for (int t = 0; t < 2; ++t) { ag[mt][t] = MFMA(bg[t], a[mt], ag[mt][t]); au[mt][t] = MFMA(bu[t], a[mt], au[mt][t]); }
        }
#pragma unroll
        for (int mt = 0; mt < 2; ++mt) {
            float hv[8];
#pragma unroll
            for (int t = 0; t < 2; ++t)
#pragma unroll
                for (int r = 0; r < 4; ++r) {
                    const float gb = rbf(ag[mt][t][r]), ub = rbf(au[mt][t][r]);
                    const float sg = rbf(1.0f / (1.0f + __expf(-gb)));
                    hv[t * 4 + r] = sg * ub;
                }
            Hl[(mt * 56 + fp) * 64 + lane] = mk8(pack2(hv[0], hv[1]), pack2(hv[2], hv[3]), pack2(hv[4], hv[5]), pack2(hv[6], hv[7]));
        }
    }
    __syncthreads();
    for (int nq = w; nq < 8; nq += 4) {
        f32x4 ab[2][4];
#pragma unroll
        for (int a = 0; a < 2; ++a)
#pragma unroll
            for (int b = 0; b < 4; ++b) ab[a][b] = (f32x4){0.f, 0.f, 0.f, 0.f};
        const bf16x8* pb = WD + (size_t)(nq * 4) * 56 * 64 + lane;
#pragma unroll 4
        for (int kk = 0; kk < 56; ++kk) {
            const bf16x8 a0 = Hl[kk * 64 + lane], a1 = Hl[(56 + kk) * 64 + lane];
#pragma unroll
            for (int t = 0; t < 4; ++t) {
                const bf16x8 b = pb[(t * 56 + kk) * 64];
                ab[0][t] = MFMA(b, a0, ab[0][t]); ab[1][t] = MFMA(b, a1, ab[1][t]);
            }
        }
#pragma unroll
        for (int mt = 0; mt < 2; ++mt)
#pragma unroll
            for (int t = 0; t < 4; ++t)
                *(v2u*)(Bout + (size_t)(mb * 32 + mt * 16 + i) * DM + (nq * 4 + t) * 16 + 4 * g) =
                    (v2u){pack2(ab[mt][t][0], ab[mt][t][1]), pack2(ab[mt][t][2], ab[mt][t][3])};
    }
}

constexpr int L_WO = 0, L_MEM = 131072, L_SC = L_MEM + 8192, L_OE = L_SC + 6144, L_TOTAL = L_OE + 2048;
constexpr unsigned SPINCAP = 1u << 20;
constexpr int NWORK = 224;
constexpr float SCALE = 0.08838834764831845f;
#define SLP1 12
#define SLP2 12

__device__ __forceinline__ float xor32_max(float x) { auto r = __builtin_amdgcn_permlane32_swap(__float_as_uint(x), __float_as_uint(x), false, false); return fmaxf(__uint_as_float(r[0]), __uint_as_float(r[1])); }
__device__ __forceinline__ float xor16_max(float x) { auto r = __builtin_amdgcn_permlane16_swap(__float_as_uint(x), __float_as_uint(x), false, false); return fmaxf(__uint_as_float(r[0]), __uint_as_float(r[1])); }
__device__ __forceinline__ float xor32_add(float x) { auto r = __builtin_amdgcn_permlane32_swap(__float_as_uint(x), __float_as_uint(x), false, false); return __uint_as_float(r[0]) + __uint_as_float(r[1]); }
__device__ __forceinline__ float xor16_add(float x) { auto r = __builtin_amdgcn_permlane16_swap(__float_as_uint(x), __float_as_uint(x), false, false); return __uint_as_float(r[0]) + __uint_as_float(r[1]); }
__device__ __forceinline__ float ror8(float x) { return __int_as_float(__builtin_amdgcn_update_dpp(0, __float_as_int(x), 0x128, 0xf, 0xf, false)); }

#define AMFMA0(d, a, b) asm volatile("s_nop 1\n\tv_mfma_f32_16x16x32_bf16 %0, %1, %2, 0" : "=&v"(d) : "v"(a), "v"(b))
#define AMFMA(d, a, b) asm volatile("s_nop 1\n\tv_mfma_f32_16x16x32_bf16 %0, %1, %2, %0" : "+v"(d) : "v"(a), "v"(b))
#define AMFMA16(d, a, b) asm volatile("s_nop 1\n\tv_mfma_f32_16x16x16_bf16 %0, %1, %2, %0" : "+v"(d) : "v"(a), "v"(b))

__global__ __launch_bounds__(256, 1) void k_chain(const u16* __restrict__ mem0, const bf16x8* __restrict__ WT, const bf16x8* __restrict__ WOT,
                                                  const u16* __restrict__ KX, const u16* __restrict__ VXT, u16* __restrict__ KM,
                                                  u16* __restrict__ VM, unsigned* Pbuf, unsigned* Mbuf, unsigned* tmo,
                                                  const bf16x8* __restrict__ XN, const bf16x8* __restrict__ WG, const bf16x8* __restrict__ WU,
                                                  const bf16x8* __restrict__ WD, u16* __restrict__ QX, u16* __restrict__ Bout) {
    extern __shared__ __attribute__((aligned(16))) char lds[];
    const int h = blockIdx.x;
    const int w = __builtin_amdgcn_readfirstlane(threadIdx.x >> 6), lane = threadIdx.x & 63, g = lane >> 4, i = lane & 15;
    if (blockIdx.x >= NH) {
        const int wb = blockIdx.x - NH;
        if (wb < 64) { glu_block(XN, WG, WU, WD, Bout, lds, wb, w, lane); return; }
        for (int t = (wb - 64) * 4 + w; t < 2048; t += (NWORK - 64) * 4) qkv_task(XN, WT, QX, nullptr, nullptr, t >> 6, 0, (t >> 1) & 31, t & 1, lane);
        return;
    }
    const __amdgpu_buffer_rsrc_t prs = __builtin_amdgcn_make_buffer_rsrc(Pbuf, 0, 32 * 16384, 0x00020000);
    const __amdgpu_buffer_rsrc_t mrs = __builtin_amdgcn_make_buffer_rsrc(Mbuf, 0, 64 * 8192, 0x00020000);

    bf16x8 WA[64], WV[32];
    {
        const bf16x8* src = WT + (size_t)(h * 4 + w) * 96 * 64 + lane;
#pragma unroll
        for (int f = 0; f < 64; ++f) WA[f] = src[f * 64];
#pragma unroll
        for (int f = 0; f < 32; ++f) WV[f] = src[(64 + f) * 64];
        const bf16x8* so = WOT + ((size_t)h * 128 + w * 32) * 64 + lane;
        bf16x8* lo = (bf16x8*)(lds + L_WO) + (w * 32) * 64 + lane;
#pragma unroll 8
        for (int f = 0; f < 32; ++f) lo[f * 64] = so[f * 64];
    }
    {
        const int tid = threadIdx.x;
#pragma unroll
        for (int c = tid; c < 512; c += 256) {
            const int t = c & 7, gg = (c >> 3) & 3, kk = c >> 5;
            *(v4u*)(lds + L_MEM + c * 16) = *(const v4u*)(mem0 + t * DM + 32 * kk + 8 * gg);
        }
    }
    v4u pk[2], pv[2];
    {
        const char* kb = (const char*)(KX + ((size_t)0 * NH + h) * 4096) + w * 1024 + lane * 16;
        const char* vb = (const char*)(VXT + ((size_t)0 * NH + h) * 4096) + w * 2048 + lane * 16;
        pk[0] = *(const v4u*)kb; pk[1] = *(const v4u*)(kb + 4096);
        pv[0] = *(const v4u*)vb; pv[1] = *(const v4u*)(vb + 1024);
    }
    const int red = h * 4 + w;
#define STAMP(k)
    for (int s = 0; s < NSEG; ++s) {
        __syncthreads();
        STAMP(0);
        asm volatile("" : "+v"(pk[0]), "+v"(pk[1]), "+v"(pv[0]), "+v"(pv[1]));
        f32x4 acc[6];
        bf16x8 mf[16];
        const char* mp = lds + L_MEM + g * 128 + (i & 7) * 16;
        {
#pragma unroll
            for (int kk = 0; kk < 4; ++kk) mf[kk] = *(const bf16x8*)(mp + kk * 512);
#pragma unroll
            for (int kk = 0; kk < 16; ++kk) {
                if (kk + 4 < 16) mf[kk + 4] = *(const bf16x8*)(mp + (kk + 4) * 512);
                if (kk == 0) {
                    asm volatile("v_mfma_f32_16x16x32_bf16 %0, %4, %8, 0\n\tv_mfma_f32_16x16x32_bf16 %1, %5, %8, 0\n\t"
                                 "v_mfma_f32_16x16x32_bf16 %2, %6, %8, 0\n\tv_mfma_f32_16x16x32_bf16 %3, %7, %8, 0"
                                 : "=&v"(acc[0]), "=&v"(acc[1]), "=&v"(acc[2]), "=&v"(acc[3])
                                 : "a"(WA[0]), "a"(WA[16]), "a"(WA[32]), "a"(WA[48]), "v"(mf[0]));
                } else {
                    asm volatile("v_mfma_f32_16x16x32_bf16 %0, %4, %8, %0\n\tv_mfma_f32_16x16x32_bf16 %1, %5, %8, %1\n\t"
                                 "v_mfma_f32_16x16x32_bf16 %2, %6, %8, %2\n\tv_mfma_f32_16x16x32_bf16 %3, %7, %8, %3"
                                 : "+v"(acc[0]), "+v"(acc[1]), "+v"(acc[2]), "+v"(acc[3])
                                 : "a"(WA[kk]), "a"(WA[16 + kk]), "a"(WA[32 + kk]), "a"(WA[48 + kk]), "v"(mf[kk]));
                }
            }
            asm volatile("s_nop 7\n\ts_nop 1" : "+v"(acc[0]), "+v"(acc[1]), "+v"(acc[2]), "+v"(acc[3]));
        }
        const bf16x8 qf = mk8(pack2(acc[0][0], acc[0][1]), pack2(acc[0][2], acc[0][3]), pack2(acc[1][0], acc[1][1]), pack2(acc[1][2], acc[1][3]));
        const bf16x8 kf = mk8(pack2(acc[2][0], acc[2][1]), pack2(acc[2][2], acc[2][3]), pack2(acc[3][0], acc[3][1]), pack2(acc[3][2], acc[3][3]));
        if (i < 8) *(bf16x8*)(KM + (((size_t)s * NH + h) * MT + i) * DH + 32 * w + 8 * g) = kf;
        if (s < NSEG - 1) {
            f32x4 sp[3];
            AMFMA0(sp[0], kf, qf); AMFMA0(sp[1], pk[0], qf); AMFMA0(sp[2], pk[1], qf);
            asm volatile("s_nop 7" : "+v"(sp[0]), "+v"(sp[1]), "+v"(sp[2]));
            if (i < 8) {
#pragma unroll
                for (int a = 0; a < 3; ++a) *(f32x4*)(lds + L_SC + (((w * 3 + a) * 4 + g) * 8 + i) * 16) = sp[a];
            }
        }
        {
#pragma unroll
            for (int kk = 0; kk < 4; ++kk) mf[kk] = *(const bf16x8*)(mp + kk * 512);
#pragma unroll
            for (int kk = 0; kk < 16; ++kk) {
                if (kk + 4 < 16) mf[kk + 4] = *(const bf16x8*)(mp + (kk + 4) * 512);
                if (kk == 0) {
                    asm volatile("v_mfma_f32_16x16x32_bf16 %0, %4, %2, 0\n\tv_mfma_f32_16x16x32_bf16 %1, %4, %3, 0"
                                 : "=&v"(acc[4]), "=&v"(acc[5]) : "v"(WV[0]), "v"(WV[16]), "v"(mf[0]));
                } else {
                    asm volatile("v_mfma_f32_16x16x32_bf16 %0, %4, %2, %0\n\tv_mfma_f32_16x16x32_bf16 %1, %4, %3, %1"
                                 : "+v"(acc[4]), "+v"(acc[5]) : "v"(WV[kk]), "v"(WV[16 + kk]), "v"(mf[kk]));
                }
            }
            asm volatile("s_nop 7\n\ts_nop 1" : "+v"(acc[4]), "+v"(acc[5]));
        }
        const v2u v0 = {pack2(acc[4][0], acc[4][1]), pack2(acc[4][2], acc[4][3])};
        const v2u v1 = {pack2(acc[5][0], acc[5][1]), pack2(acc[5][2], acc[5][3])};
        if (g < 2) {
            *(v2u*)(VM + ((((size_t)s * NH + h) * 8 + 2 * w) * 32 + lane) * 4) = v0;
            *(v2u*)(VM + ((((size_t)s * NH + h) * 8 + 2 * w + 1) * 32 + lane) * 4) = v1;
        }
        if (s == NSEG - 1) break;
        STAMP(1);
        __syncthreads();
        STAMP(2);
        {
            f32x4 pp[4][3];
#pragma unroll
            for (int ww = 0; ww < 4; ++ww)
#pragma unroll
                for (int a = 0; a < 3; ++a) pp[ww][a] = *(const f32x4*)(lds + L_SC + (((ww * 3 + a) * 4 + g) * 8 + (i & 7)) * 16);
            float sv[12];
#pragma unroll
            for (int a = 0; a < 3; ++a)
#pragma unroll
                for (int r = 0; r < 4; ++r) sv[a * 4 + r] = rbf(rbf((pp[0][a][r] + pp[1][a][r]) + (pp[2][a][r] + pp[3][a][r])) * SCALE);
            const bool mv = g < 2;
            float mx = fmaxf(fmaxf(fmaxf(sv[4], sv[5]), fmaxf(sv[6], sv[7])), fmaxf(fmaxf(sv[8], sv[9]), fmaxf(sv[10], sv[11])));
            const float mxm = fmaxf(fmaxf(sv[0], sv[1]), fmaxf(sv[2], sv[3]));
            mx = mv ? fmaxf(mx, mxm) : mx;
            mx = xor16_max(mx);
            mx = xor32_max(mx);
            float ex[12], sum = 0.f;
#pragma unroll
            for (int a = 0; a < 12; ++a) { ex[a] = __expf(sv[a] - mx); if (a < 4) ex[a] = mv ? ex[a] : 0.f; sum += ex[a]; }
            sum = xor16_add(sum);
            sum = xor32_add(sum);
            const float inv = 1.0f / sum;
            const bf16x8 pX = mk8(pack2(ex[4] * inv, ex[5] * inv), pack2(ex[6] * inv, ex[7] * inv), pack2(ex[8] * inv, ex[9] * inv), pack2(ex[10] * inv, ex[11] * inv));
            const v2u pM = {pack2(ex[0] * inv, ex[1] * inv), pack2(ex[2] * inv, ex[3] * inv)};
            f32x4 o0, o1;
            AMFMA0(o0, pv[0], pX); AMFMA0(o1, pv[1], pX);
            asm volatile("s_nop 5" : "+v"(o0), "+v"(o1));
            AMFMA16(o0, v0, pM); AMFMA16(o1, v1, pM);
            asm volatile("s_nop 7" : "+v"(o0), "+v"(o1));
            if (i < 8) *(bf16x8*)(lds + L_OE + ((w * 4 + g) * 8 + i) * 16) = mk8(pack2(o0[0], o0[1]), pack2(o0[2], o0[3]), pack2(o1[0], o1[1]), pack2(o1[2], o1[3]));
        }
        const unsigned tag = 1u + (unsigned)(s % 3);
        {
            const char* wp = lds + L_WO + (w * 32) * 1024 + lane * 16;
            bf16x8 wf[2][8];
#pragma unroll
            for (int f = 0; f < 8; ++f) wf[0][f] = *(const bf16x8*)(wp + f * 1024);
            __syncthreads();
            STAMP(3);
            bf16x8 of[4];
#pragma unroll
            for (int ks = 0; ks < 4; ++ks) of[ks] = *(const bf16x8*)(lds + L_OE + ((ks * 4 + g) * 8 + (i & 7)) * 16);
#pragma unroll
            for (int a = 0; a < 4; ++a) {
                if (a < 3) {
#pragma unroll
                    for (int f = 0; f < 8; ++f) wf[(a + 1) & 1][f] = *(const bf16x8*)(wp + ((a + 1) * 8 + f) * 1024);
                }
                f32x4 d0, d1;
                AMFMA0(d0, wf[a & 1][0], of[0]); AMFMA0(d1, wf[a & 1][4], of[0]);
#pragma unroll
                for (int ks = 1; ks < 4; ++ks) { AMFMA(d0, wf[a & 1][ks], of[ks]); AMFMA(d1, wf[a & 1][4 + ks], of[ks]); }
                asm volatile("s_nop 7" : "+v"(d0), "+v"(d1));
                v4u v;
#pragma unroll
                for (int r = 0; r < 4; ++r) {
                    const float x1 = ror8(d1[r]);
                    v[r] = (__float_as_uint(i < 8 ? d0[r] : x1) & ~3u) | tag;
                }
                const int T = 8 * w + 2 * a + (i >> 3);
                __builtin_amdgcn_raw_buffer_store_b128(v, prs, (unsigned)(h * 16384 + (4 * T + g) * 128 + (i & 7) * 16), 0, 16);
            }
        }
        STAMP(4);
        {
            const char* kb = (const char*)(KX + ((size_t)(s + 1) * NH + h) * 4096) + w * 1024 + lane * 16;
            const char* vb = (const char*)(VXT + ((size_t)(s + 1) * NH + h) * 4096) + w * 2048 + lane * 16;
            pk[0] = *(const v4u*)kb; pk[1] = *(const v4u*)(kb + 4096);
            pv[0] = *(const v4u*)vb; pv[1] = *(const v4u*)(vb + 1024);
        }
        for (int z = 0; z < SLP1; ++z) __builtin_amdgcn_s_sleep(1);
        {
            v4u r[4];
            unsigned spins = 0;
            for (;;) {
#pragma unroll
                for (int q = 0; q < 4; ++q)
                    r[q] = __builtin_amdgcn_raw_buffer_load_b128(prs, (unsigned)((q * 8 + (lane >> 3)) * 16384 + red * 128 + (lane & 7) * 16), 0, 16);
                bool ok = true;
#pragma unroll
                for (int q = 0; q < 4; ++q) ok &= ((r[q].x & 3u) == tag) & ((r[q].y & 3u) == tag) & ((r[q].z & 3u) == tag) & ((r[q].w & 3u) == tag);
                if (__all(ok)) break;
                if (++spins > SPINCAP) { if (lane == 0) atomicAdd(tmo, 1u); break; }
            }
            STAMP(5);
            float sm[4];
#pragma unroll
            for (int c = 0; c < 4; ++c) {
                sm[c] = (__uint_as_float(r[0][c] & ~3u) + __uint_as_float(r[1][c] & ~3u)) + (__uint_as_float(r[2][c] & ~3u) + __uint_as_float(r[3][c] & ~3u));
                sm[c] += ror8(sm[c]);
                sm[c] = xor16_add(sm[c]);
                sm[c] = xor32_add(sm[c]);
            }
            if (lane < 8) {
                v2u v = {pack2(sm[0], sm[1]), pack2(sm[2], sm[3])};
                if (v.x == 0u) v.x = 0x8000u;
                if (v.y == 0u) v.y = 0x8000u;
                __builtin_amdgcn_raw_buffer_store_b64(v, mrs, (unsigned)(s * 8192 + red * 64 + lane * 8), 0, 16);
            }
        }
        STAMP(6);
        for (int z = 0; z < SLP2; ++z) __builtin_amdgcn_s_sleep(1);
        {
            v4u m[2];
            unsigned spins = 0;
            for (;;) {
#pragma unroll
                for (int q = 0; q < 2; ++q) m[q] = __builtin_amdgcn_raw_buffer_load_b128(mrs, (unsigned)(s * 8192 + w * 2048 + q * 1024 + lane * 16), 0, 16);
                bool ok = true;
#pragma unroll
                for (int q = 0; q < 2; ++q) ok &= (m[q].x != 0u) & (m[q].y != 0u) & (m[q].z != 0u) & (m[q].w != 0u);
                if (__all(ok)) break;
                if (++spins > SPINCAP) { if (lane == 0) atomicAdd(tmo, 1u); break; }
            }
#pragma unroll
            for (int q = 0; q < 2; ++q) {
                const int R = w * 32 + q * 16 + (lane >> 2), t = 2 * (lane & 3);
                char* dst = lds + L_MEM + (R >> 3) * 512 + ((R >> 1) & 3) * 128 + t * 16 + (R & 1) * 8;
                *(v2u*)dst = (v2u){m[q].x, m[q].y};
                *(v2u*)(dst + 16) = (v2u){m[q].z, m[q].w};
            }
        }
        STAMP(7);
    }
}

__global__ __launch_bounds__(256) void k_xattn(const u16* __restrict__ QX, const u16* __restrict__ KX, const u16* __restrict__ VXT,
                                               const u16* __restrict__ KM, const u16* __restrict__ VM, bf16x8* __restrict__ Oft) {
    const int wid = blockIdx.x * 4 + (threadIdx.x >> 6), lane = threadIdx.x & 63, g = lane >> 4, i = lane & 15;
    const int s = wid >> 5, h = wid & 31;
    const size_t sh = (size_t)s * NH + h;
    const u16* qb = QX + sh * 4096; const u16* kb = KX + sh * 4096; const u16* vb = VXT + sh * 4096;
    const u16* kmb = KM + sh * 1024; const u16* vmb = VM + sh * 1024;
    bf16x8 kf[4], k1[4], k2[4];
#pragma unroll
    for (int ks = 0; ks < 4; ++ks) {
        kf[ks] = *(const bf16x8*)(kmb + (i & 7) * DH + 32 * ks + 8 * g);
        k1[ks] = *(const bf16x8*)(kb + (ks * 64 + lane) * 8);
        k2[ks] = *(const bf16x8*)(kb + ((4 + ks) * 64 + lane) * 8);
    }
#pragma unroll
    for (int qt = 0; qt < 2; ++qt) {
        f32x4 sc[3];
#pragma unroll
        for (int a = 0; a < 3; ++a) sc[a] = (f32x4){0.f, 0.f, 0.f, 0.f};
#pragma unroll
        for (int ks = 0; ks < 4; ++ks) {
            const bf16x8 qf = *(const bf16x8*)(qb + ((qt * 4 + ks) * 64 + lane) * 8);
            sc[0] = MFMA(kf[ks], qf, sc[0]);
            sc[1] = MFMA(k1[ks], qf, sc[1]);
            sc[2] = MFMA(k2[ks], qf, sc[2]);
        }
        const int tq = qt * 16 + i;
        float sv[12]; bool ok[12];
#pragma unroll
        for (int r = 0; r < 4; ++r) { ok[r] = g < 2; ok[4 + r] = (8 * g + r) <= tq; ok[8 + r] = (8 * g + 4 + r) <= tq; }
        float mx = -3.0e38f;
#pragma unroll
        for (int a = 0; a < 3; ++a)
#pragma unroll
            for (int r = 0; r < 4; ++r) { sv[a * 4 + r] = rbf(rbf(sc[a][r]) * SCALE); mx = ok[a * 4 + r] ? fmaxf(mx, sv[a * 4 + r]) : mx; }
        mx = fmaxf(mx, __shfl_xor(mx, 16));
        mx = fmaxf(mx, __shfl_xor(mx, 32));
        float ex[12], sum = 0.f;
#pragma unroll
        for (int a = 0; a < 12; ++a) { ex[a] = ok[a] ? __expf(sv[a] - mx) : 0.f; sum += ex[a]; }
        sum += __shfl_xor(sum, 16);
        sum += __shfl_xor(sum, 32);
        const float inv = 1.0f / sum;
        const bf16x8 pX = mk8(pack2(ex[4] * inv, ex[5] * inv), pack2(ex[6] * inv, ex[7] * inv), pack2(ex[8] * inv, ex[9] * inv), pack2(ex[10] * inv, ex[11] * inv));
        const bf16x8 pM = mk8(pack2(ex[0] * inv, ex[1] * inv), pack2(ex[2] * inv, ex[3] * inv), 0u, 0u);
#pragma unroll
        for (int ks = 0; ks < 4; ++ks) {
            f32x4 o[2];
#pragma unroll
            for (int t = 0; t < 2; ++t) {
                const int tau = 2 * ks + t;
                const bf16x8 vx = *(const bf16x8*)(vb + (tau * 64 + lane) * 8);
                const v2u vm = *(const v2u*)(vmb + (tau * 32 + (lane & 31)) * 4);
                o[t] = MFMA(vx, pX, ((f32x4){0.f, 0.f, 0.f, 0.f}));
                o[t] = MFMA(mk8(vm.x, vm.y, 0u, 0u), pM, o[t]);
            }
            Oft[((size_t)(s * 2 + qt) * 128 + h * 4 + ks) * 64 + lane] =
                mk8(pack2(o[0][0], o[0][1]), pack2(o[0][2], o[0][3]), pack2(o[1][0], o[1][1]), pack2(o[1][2], o[1][3]));
        }
    }
}

__global__ __launch_bounds__(256) void k_final(const bf16x8* __restrict__ Oft, const bf16x8* __restrict__ WOT, const u16* __restrict__ Bout,
                                               const u16* __restrict__ x, u16* __restrict__ out) {
    __shared__ f32x4 red[3][16][64];
    const int w = threadIdx.x >> 6, lane = threadIdx.x & 63, g = lane >> 4, i = lane & 15;
    const int nb = blockIdx.x & 7, mb = blockIdx.x >> 3;
    f32x4 acc[4][4];
#pragma unroll
    for (int a = 0; a < 4; ++a)
#pragma unroll
        for (int b = 0; b < 4; ++b) acc[a][b] = (f32x4){0.f, 0.f, 0.f, 0.f};
    const bf16x8* pa = Oft + ((size_t)(mb * 4) * 128 + w * 32) * 64 + lane;
    const bf16x8* pb = WOT + ((size_t)(w * 8) * 32 + nb * 4) * 4 * 64 + lane;
#pragma unroll 2
    for (int kk = 0; kk < 32; ++kk) {
        bf16x8 a[4], b[4];
#pragma unroll
        for (int t = 0; t < 4; ++t) { a[t] = pa[(t * 128 + kk) * 64]; b[t] = pb[(((kk >> 2) * 32 + t) * 4 + (kk & 3)) * 64]; }
#pragma unroll
        for (int mt = 0; mt < 4; ++mt)
#pragma unroll
            for (int t = 0; t < 4; ++t) acc[mt][t] = MFMA(b[t], a[mt], acc[mt][t]);
    }
    if (w > 0) {
#pragma unroll
        for (int mt = 0; mt < 4; ++mt)
#pragma unroll
            for (int t = 0; t < 4; ++t) red[w - 1][mt * 4 + t][lane] = acc[mt][t];
    }
    __syncthreads();
    if (w == 0) {
#pragma unroll
        for (int mt = 0; mt < 4; ++mt)
#pragma unroll
            for (int t = 0; t < 4; ++t) {
                const f32x4 sacc = (acc[mt][t] + red[0][mt * 4 + t][lane]) + (red[1][mt * 4 + t][lane] + red[2][mt * 4 + t][lane]);
                const size_t idx = (size_t)(mb * 64 + mt * 16 + i) * DM + (nb * 4 + t) * 16 + 4 * g;
                const v2u xr = *(const v2u*)(x + idx), br = *(const v2u*)(Bout + idx);
                const float xv[4] = {__uint_as_float(xr.x << 16), __uint_as_float(xr.x & 0xffff0000u), __uint_as_float(xr.y << 16), __uint_as_float(xr.y & 0xffff0000u)};
                const float bv[4] = {__uint_as_float(br.x << 16), __uint_as_float(br.x & 0xffff0000u), __uint_as_float(br.y << 16), __uint_as_float(br.y & 0xffff0000u)};
                float y[4];
#pragma unroll
                for (int r = 0; r < 4; ++r) y[r] = rbf(xv[r] + rbf(sacc[r])) + bv[r];
                *(v2u*)(out + idx) = (v2u){pack2(y[0], y[1]), pack2(y[2], y[3])};
            }
    }
}

extern "C" void kernel_launch(void* const* d_in, const int* in_sizes, int n_in, void* d_out, int out_size, void* d_ws, size_t ws_size,
                              hipStream_t stream) {
    const u16* x = (const u16*)d_in[0];
    const u16* mem0 = (const u16*)d_in[1];
    const u16* Wq = (const u16*)d_in[2];
    const u16* Wk = (const u16*)d_in[3];
    const u16* Wv = (const u16*)d_in[4];
    const u16* Wo = (const u16*)d_in[5];
    const u16* Wg = (const u16*)d_in[8];
    const u16* Wu = (const u16*)d_in[9];
    const u16* Wd = (const u16*)d_in[10];
    char* ws = (char*)d_ws;
    (void)hipFuncSetAttribute((const void*)k_chain, hipFuncAttributeMaxDynamicSharedMemorySize, L_TOTAL);
    (void)hipMemsetAsync(ws, 0, ZERO_BYTES, stream);
    k_prep<<<PREP_LN_BLOCKS + (TILE_BLOCKS + 255) / 256, 256, 0, stream>>>(x, Wq, Wk, Wv, Wo, Wg, Wu, Wd, ws);
    k_kv<<<32 * 2 * 64 / 4, 256, 0, stream>>>((const bf16x8*)(ws + OFF_XN), (const bf16x8*)(ws + OFF_WT), (u16*)(ws + OFF_KX), (u16*)(ws + OFF_VXT));
    k_chain<<<NH + NWORK, 256, L_TOTAL, stream>>>(mem0, (const bf16x8*)(ws + OFF_WT), (const bf16x8*)(ws + OFF_WOT), (const u16*)(ws + OFF_KX),
                                                  (const u16*)(ws + OFF_VXT), (u16*)(ws + OFF_KM), (u16*)(ws + OFF_VM), (unsigned*)(ws + OFF_P),
                                                  (unsigned*)(ws + OFF_M), (unsigned*)(ws + OFF_TMO), (const bf16x8*)(ws + OFF_XN),
                                                  (const bf16x8*)(ws + OFF_WG), (const bf16x8*)(ws + OFF_WU), (const bf16x8*)(ws + OFF_WD),
                                                  (u16*)(ws + OFF_QX), (u16*)(ws + OFF_H));
    k_xattn<<<NSEG * NH / 4, 256, 0, stream>>>((const u16*)(ws + OFF_QX), (const u16*)(ws + OFF_KX), (const u16*)(ws + OFF_VXT),
                                               (const u16*)(ws + OFF_KM), (const u16*)(ws + OFF_VM), (bf16x8*)(ws + OFF_O));
    k_final<<<32 * 8, 256, 0, stream>>>((const bf16x8*)(ws + OFF_O), (const bf16x8*)(ws + OFF_WOT), (const u16*)(ws + OFF_H), x, (u16*)d_out);
}
```

```cpp
#include <hip/hip_runtime.h>
#include <hip/hip_bf16.h>

constexpr int S_ = 2048, DM = 512, NH = 32, DH = 128, MT = 8, SEG = 32, NSEG = 64, FF = 1792;

typedef unsigned short u16;
typedef __attribute__((ext_vector_type(8))) short bf16x8;
typedef __attribute__((ext_vector_type(4))) float f32x4;
typedef __attribute__((ext_vector_type(4))) unsigned v4u;
typedef __attribute__((ext_vector_type(2))) unsigned v2u;
typedef __bf16 bf16x2_t __attribute__((ext_vector_type(2)));

__device__ __forceinline__ unsigned pack2(float a, float b) {
    bf16x2_t v = {(__bf16)a, (__bf16)b};
    return __builtin_bit_cast(unsigned, v);
}
__device__ __forceinline__ float rbf(float a) { return (float)(__bf16)a; }
__device__ __forceinline__ float bf2f(u16 b) { return __uint_as_float(((unsigned)b) << 16); }
__device__ __forceinline__ bf16x8 mk8(unsigned a, unsigned b, unsigned c, unsigned d) {
    v4u v = {a, b, c, d};
    return __builtin_bit_cast(bf16x8, v);
}
#define MFMA(a, b, c) __builtin_amdgcn_mfma_f32_16x16x32_bf16((a), (b), (c), 0, 0, 0)

__device__ __forceinline__ int pi_col(int tau, int rho) { return 32 * (tau >> 1) + 8 * (rho >> 2) + 4 * (tau & 1) + (rho & 3); }

constexpr size_t OFF_TMO = 0;
constexpr size_t OFF_M   = 65536;
constexpr size_t OFF_P   = OFF_M + 64 * 8192;
constexpr size_t ZERO_BYTES = OFF_P + (size_t)63 * 32 * 16384;
constexpr size_t OFF_XN  = (ZERO_BYTES + 4095) / 4096 * 4096;
constexpr size_t OFF_WT  = OFF_XN + (size_t)S_ * DM * 2;
constexpr size_t OFF_WOT = OFF_WT + (size_t)3 * DM * NH * DH * 2;
constexpr size_t OFF_WG  = OFF_WOT + (size_t)DM * NH * DH * 2;
constexpr size_t OFF_WU  = OFF_WG + (size_t)DM * FF * 2;
constexpr size_t OFF_WD  = OFF_WU + (size_t)DM * FF * 2;
constexpr size_t OFF_QX  = OFF_WD + (size_t)DM * FF * 2;
constexpr size_t OFF_KX  = OFF_QX + (size_t)S_ * NH * DH * 2;
constexpr size_t OFF_VXT = OFF_KX + (size_t)S_ * NH * DH * 2;
constexpr size_t OFF_KM  = OFF_VXT + (size_t)S_ * NH * DH * 2;
constexpr size_t OFF_VM  = OFF_KM + (size_t)NSEG * NH * MT * DH * 2;
constexpr size_t OFF_H   = OFF_VM + (size_t)NSEG * NH * MT * DH * 2;
constexpr size_t OFF_O   = OFF_H + (size_t)S_ * FF * 2;
constexpr size_t OFF_PART = OFF_O + (size_t)S_ * NH * DH * 2;
constexpr size_t WS_TOTAL = OFF_PART + (size_t)4 * S_ * DM * 4;

__device__ __forceinline__ void ln_row(const u16* __restrict__ x, u16* __restrict__ xn_ft, int row, int lane) {
    const v4u raw = *(const v4u*)(x + (size_t)row * DM + lane * 8);
    float v[8];
    v[0] = __uint_as_float(raw.x << 16); v[1] = __uint_as_float(raw.x & 0xffff0000u);
    v[2] = __uint_as_float(raw.y << 16); v[3] = __uint_as_float(raw.y & 0xffff0000u);
    v[4] = __uint_as_float(raw.z << 16); v[5] = __uint_as_float(raw.z & 0xffff0000u);
    v[6] = __uint_as_float(raw.w << 16); v[7] = __uint_as_float(raw.w & 0xffff0000u);
    float s = 0.f;
#pragma unroll
    for (int j = 0; j < 8; ++j) s += v[j];
#pragma unroll
    for (int o = 1; o < 64; o <<= 1) s += __shfl_xor(s, o);
    const float mean = s * (1.0f / DM);
    float q = 0.f;
#pragma unroll
    for (int j = 0; j < 8; ++j) { const float d = v[j] - mean; q += d * d; }
#pragma unroll
    for (int o = 1; o < 64; o <<= 1) q += __shfl_xor(q, o);
    const float mb = rbf(mean), vb = rbf(q * (1.0f / DM));
    const float den = rbf(sqrtf(rbf(vb + 1e-05f)));
    float y[8];
#pragma unroll
    for (int j = 0; j < 8; ++j) y[j] = rbf(v[j] - mb) / den;
    v4u o4 = {pack2(y[0], y[1]), pack2(y[2], y[3]), pack2(y[4], y[5]), pack2(y[6], y[7])};
    const int mt = row >> 4, kk = lane >> 2, fl = (lane & 3) * 16 + (row & 15);
    *(v4u*)(xn_ft + (((size_t)mt * 16 + kk) * 64 + fl) * 8) = o4;
}

__device__ __forceinline__ void transpose8x8(const v4u (&r)[8], v4u (&o)[8]) {
#pragma unroll
    for (int c = 0; c < 4; ++c) {
#pragma unroll
        for (int d = 0; d < 4; ++d) {
            o[2 * c][d] = __builtin_amdgcn_perm(r[2 * d + 1][c], r[2 * d][c], 0x05040100u);
            o[2 * c + 1][d] = __builtin_amdgcn_perm(r[2 * d + 1][c], r[2 * d][c], 0x07060302u);
        }
    }
}
constexpr int B_WT = 3 * 64 * 512, B_WOT = 512 * 64, B_WG = 64 * 224, B_WD = 224 * 64;
constexpr int TILE_BLOCKS = B_WT + B_WOT + 2 * B_WG + B_WD;
__device__ __forceinline__ void tile_w_block(int c, const u16* __restrict__ Wq, const u16* __restrict__ Wk, const u16* __restrict__ Wv,
                                             const u16* __restrict__ Wo, const u16* __restrict__ Wg, const u16* __restrict__ Wu,
                                             const u16* __restrict__ Wd, char* __restrict__ ws) {
    const u16* src; int pitch; u16* d0; int dstep;
    bool perm;
    if (c < B_WT) {
        const int nb = c & 511, kb = (c >> 9) & 63, m = c >> 15;
        const int h = nb >> 4, ks = (nb >> 2) & 3, gp = nb & 3, kk = kb >> 2, g = kb & 3;
        src = (m == 0 ? Wq : (m == 1 ? Wk : Wv)) + (size_t)(8 * kb) * 4096 + 8 * nb; pitch = 4096;
        d0 = (u16*)(ws + OFF_WT) + ((size_t)((h * 4 + ks) * 96 + m * 32 + kk) * 64 + g * 16 + 4 * gp) * 8; dstep = 16 * 64 * 8; perm = true;
    } else if ((c -= B_WT) < B_WOT) {
        const int nb = c & 63, kb = c >> 6, h = kb >> 4, ks = (kb >> 2) & 3, g = kb & 3, T = nb >> 1;
        src = Wo + (size_t)(8 * kb) * 512 + 8 * nb; pitch = 512;
        d0 = (u16*)(ws + OFF_WOT) + ((size_t)((h * 32 + T) * 4 + ks) * 64 + g * 16 + 8 * (nb & 1)) * 8; dstep = 0; perm = false;
    } else if ((c -= B_WOT) < 2 * B_WG) {
        const int which = c >= B_WG; if (which) c -= B_WG;
        const int nb = c % 224, kb = c / 224, fp = nb >> 2, gp = nb & 3, kk = kb >> 2, g = kb & 3;
        src = (which ? Wu : Wg) + (size_t)(8 * kb) * FF + 8 * nb; pitch = FF;
        d0 = (u16*)(ws + (which ? OFF_WU : OFF_WG)) + ((size_t)((2 * fp) * 16 + kk) * 64 + g * 16 + 4 * gp) * 8; dstep = 16 * 64 * 8; perm = true;
    } else {
        c -= 2 * B_WG;
        const int nb = c & 63, kb = c >> 6, kk = kb >> 2, g = kb & 3, nt = nb >> 1;
        src = Wd + (size_t)(8 * kb) * 512 + 8 * nb; pitch = 512;
        d0 = (u16*)(ws + OFF_WD) + ((size_t)(nt * 56 + kk) * 64 + g * 16 + 8 * (nb & 1)) * 8; dstep = 0; perm = false;
    }
    v4u r[8], o[8];
#pragma unroll
    for (int j = 0; j < 8; ++j) r[j] = *(const v4u*)(src + (size_t)j * pitch);
    transpose8x8(r, o);
#pragma unroll
    for (int j = 0; j < 8; ++j) *(v4u*)(perm ? d0 + (j >> 2) * dstep + (j & 3) * 8 : d0 + j * 8) = o[j];
}

constexpr int PREP_LN_BLOCKS = S_ / 4, PREP_TILE_BLOCKS = (TILE_BLOCKS + 255) / 256, PREP_ZERO_BLOCKS = 1024;
__global__ __launch_bounds__(256) void k_prep(const u16* __restrict__ x, const u16* __restrict__ Wq, const u16* __restrict__ Wk,
                                              const u16* __restrict__ Wv, const u16* __restrict__ Wo, const u16* __restrict__ Wg,
                                              const u16* __restrict__ Wu, const u16* __restrict__ Wd, char* __restrict__ ws) {
    if (blockIdx.x < PREP_LN_BLOCKS) { ln_row(x, (u16*)(ws + OFF_XN), blockIdx.x * 4 + (threadIdx.x >> 6), threadIdx.x & 63); return; }
    if (blockIdx.x < PREP_LN_BLOCKS + PREP_TILE_BLOCKS) {
        const int c = (blockIdx.x - PREP_LN_BLOCKS) * 256 + threadIdx.x;
        if (c < TILE_BLOCKS) tile_w_block(c, Wq, Wk, Wv, Wo, Wg, Wu, Wd, ws);
        return;
    }
    v4u* z = (v4u*)ws;
    for (size_t e = (size_t)(blockIdx.x - PREP_LN_BLOCKS - PREP_TILE_BLOCKS) * 256 + threadIdx.x; e < ZERO_BYTES / 16; e += (size_t)PREP_ZERO_BLOCKS * 256)
        z[e] = (v4u){0u, 0u, 0u, 0u};
}

#define GLDS16(gp, lp) __builtin_amdgcn_global_load_lds((const unsigned*)(gp), (unsigned*)(lp), 16, 0, 0)
constexpr unsigned QKV_MAT_BYTES = (unsigned)S_ * NH * DH * 2;
__device__ __forceinline__ void qkv_tile(char* lds, const bf16x8* __restrict__ XN, const bf16x8* __restrict__ WT, __amdgpu_buffer_rsrc_t qrs,
                                         int mtile, int m, int h, int w, int lane) {
    bf16x8 (*sm)[32][64] = (bf16x8 (*)[32][64])lds;
    const int wm = w >> 1, wn = w & 1, g = lane >> 4, i = lane & 15;
    const bf16x8* sp = w < 2 ? XN + ((size_t)(mtile * 8 + w * 4) * 16) * 64 + lane
                             : WT + ((size_t)(h * 4 + (w - 2) * 2) * 96 + m * 32) * 64 + lane;
    auto stage = [&](int c, int buf) {
#pragma unroll
        for (int f = 0; f < 8; ++f) {
            const int t = f >> 1, kk = 2 * c + (f & 1);
            const int off = w < 2 ? (t * 16 + kk) * 64 : ((t >> 1) * 96 + (t & 1) * 16 + kk) * 64;
            GLDS16(sp + off, &sm[buf][w * 8 + f][0]);
        }
    };
    f32x4 acc[4][4];
#pragma unroll
    for (int a = 0; a < 4; ++a)
#pragma unroll
        for (int b = 0; b < 4; ++b) acc[a][b] = (f32x4){0.f, 0.f, 0.f, 0.f};
    __syncthreads();
    stage(0, 0);
    for (int c = 0; c < 8; ++c) {
        asm volatile("s_waitcnt vmcnt(0)" ::: "memory");
        __syncthreads();
        if (c + 1 < 8) stage(c + 1, (c + 1) & 1);
#pragma unroll
        for (int k2 = 0; k2 < 2; ++k2) {
            bf16x8 a[4], b[4];
#pragma unroll
            for (int t = 0; t < 4; ++t) { a[t] = sm[c & 1][(wm * 4 + t) * 2 + k2][lane]; b[t] = sm[c & 1][16 + (wn * 4 + t) * 2 + k2][lane]; }
            if (m < 2) {
#pragma unroll
                for (int mt = 0; mt < 4; ++mt)
#pragma unroll
                    for (int t = 0; t < 4; ++t) acc[mt][t] = MFMA(b[t], a[mt], acc[mt][t]);
            } else {
#pragma unroll
                for (int mt = 0; mt < 4; ++mt)
#pragma unroll
                    for (int t = 0; t < 4; ++t) acc[mt][t] = MFMA(a[mt], b[t], acc[mt][t]);
            }
        }
    }
    const int mb = mtile * 2 + wm;
    if (m < 2) {
#pragma unroll
        for (int mt = 0; mt < 4; ++mt) {
            const int tok = mb * 64 + mt * 16 + i, s = tok >> 5, tl = tok & 31;
            const int tile = m == 0 ? (tl >> 4) : ((tl >> 2) & 1), row = m == 0 ? (tl & 15) : (4 * (tl >> 3) + (tl & 3));
#pragma unroll
            for (int p = 0; p < 2; ++p) {
                const int ks = 2 * wn + p;
                v4u o = {pack2(acc[mt][2 * p][0], acc[mt][2 * p][1]), pack2(acc[mt][2 * p][2], acc[mt][2 * p][3]),
                         pack2(acc[mt][2 * p + 1][0], acc[mt][2 * p + 1][1]), pack2(acc[mt][2 * p + 1][2], acc[mt][2 * p + 1][3])};
                __builtin_amdgcn_raw_buffer_store_b128(o, qrs, (unsigned)m * QKV_MAT_BYTES + (unsigned)((s * NH + h) * 8192 + ((tile * 4 + ks) * 64 + g * 16 + row) * 16), 0, 16);
            }
        }
    } else {
#pragma unroll
        for (int mt = 0; mt < 4; ++mt) {
            const int s = mb * 2 + (mt >> 1), gp = 2 * (mt & 1) + (g >> 1);
#pragma unroll
            for (int t = 0; t < 4; ++t) {
                const int tau = 4 * wn + t;
                v2u o = {pack2(acc[mt][t][0], acc[mt][t][1]), pack2(acc[mt][t][2], acc[mt][t][3])};
                __builtin_amdgcn_raw_buffer_store_b64(o, qrs, 2u * QKV_MAT_BYTES + (unsigned)((s * NH + h) * 8192 + (tau * 64 + gp * 16 + i) * 16 + (g & 1) * 8), 0, 16);
            }
        }
    }
}

__device__ __forceinline__ void glu_block(const bf16x8* __restrict__ XN, const bf16x8* __restrict__ WG, const bf16x8* __restrict__ WU,
                                          const bf16x8* __restrict__ WD, u16* __restrict__ Bout, char* lds, int mb, int w, int lane) {
    const int g = lane >> 4, i = lane & 15;
    bf16x8* Hl = (bf16x8*)lds;
    const bf16x8* xa = XN + (size_t)(mb * 2) * 16 * 64 + lane;
    for (int fp = w; fp < 56; fp += 4) {
        f32x4 ag[2][2], au[2][2];
#pragma unroll
        for (int a = 0; a < 2; ++a)
#pragma unroll
            for (int b = 0; b < 2; ++b) { ag[a][b] = (f32x4){0.f, 0.f, 0.f, 0.f}; au[a][b] = (f32x4){0.f, 0.f, 0.f, 0.f}; }
        const bf16x8* wg = WG + (size_t)(fp * 2) * 16 * 64 + lane;
        const bf16x8* wu = WU + (size_t)(fp * 2) * 16 * 64 + lane;
#pragma unroll 4
        for (int kk = 0; kk < 16; ++kk) {
            bf16x8 a[2], bg[2], bu[2];
#pragma unroll
            for (int t = 0; t < 2; ++t) { a[t] = xa[(t * 16 + kk) * 64]; bg[t] = wg[(t * 16 + kk) * 64]; bu[t] = wu[(t * 16 + kk) * 64]; }
#pragma unroll
            for (int mt = 0; mt < 2; ++mt)
#pragma unroll
                for (int t = 0; t < 2; ++t) { ag[mt][t] = MFMA(bg[t], a[mt], ag[mt][t]); au[mt][t] = MFMA(bu[t], a[mt], au[mt][t]); }
        }
#pragma unroll
        for (int mt = 0; mt < 2; ++mt) {
            float hv[8];
#pragma unroll
            for (int t = 0; t < 2; ++t)
#pragma unroll
                for (int r = 0; r < 4; ++r) {
                    const float gb = rbf(ag[mt][t][r]), ub = rbf(au[mt][t][r]);
                    const float sg = rbf(1.0f / (1.0f + __expf(-gb)));
                    hv[t * 4 + r] = sg * ub;
                }
            Hl[(mt * 56 + fp) * 64 + lane] = mk8(pack2(hv[0], hv[1]), pack2(hv[2], hv[3]), pack2(hv[4], hv[5]), pack2(hv[6], hv[7]));
        }
    }
    __syncthreads();
    for (int nq = w; nq < 8; nq += 4) {
        f32x4 ab[2][4];
#pragma unroll
        for (int a = 0; a < 2; ++a)
#pragma unroll
            for (int b = 0; b < 4; ++b) ab[a][b] = (f32x4){0.f, 0.f, 0.f, 0.f};
        const bf16x8* pb = WD + (size_t)(nq * 4) * 56 * 64 + lane;
#pragma unroll 4
        for (int kk = 0; kk < 56; ++kk) {
            const bf16x8 a0 = Hl[kk * 64 + lane], a1 = Hl[(56 + kk) * 64 + lane];
#pragma unroll
            for (int t = 0; t < 4; ++t) {
                const bf16x8 b = pb[(t * 56 + kk) * 64];
                ab[0][t] = MFMA(b, a0, ab[0][t]); ab[1][t] = MFMA(b, a1, ab[1][t]);
            }
        }
#pragma unroll
        for (int mt = 0; mt < 2; ++mt)
#pragma unroll
            for (int t = 0; t < 4; ++t)
                *(v2u*)(Bout + (size_t)(mb * 32 + mt * 16 + i) * DM + (nq * 4 + t) * 16 + 4 * g) =
                    (v2u){pack2(ab[mt][t][0], ab[mt][t][1]), pack2(ab[mt][t][2], ab[mt][t][3])};
    }
}

constexpr int L_WO = 0, L_MEM = 131072, L_SC = L_MEM + 8192, L_OE = L_SC + 6144, L_TOTAL = L_OE + 2048;
constexpr unsigned SPINCAP = 1u << 20;
constexpr int NWORK = 224;
constexpr float SCALE = 0.08838834764831845f;
#define SLP1 12
#define SLP2 16

__device__ __forceinline__ float xor32_max(float x) { auto r = __builtin_amdgcn_permlane32_swap(__float_as_uint(x), __float_as_uint(x), false, false); return fmaxf(__uint_as_float(r[0]), __uint_as_float(r[1])); }
__device__ __forceinline__ float xor16_max(float x) { auto r = __builtin_amdgcn_permlane16_swap(__float_as_uint(x), __float_as_uint(x), false, false); return fmaxf(__uint_as_float(r[0]), __uint_as_float(r[1])); }
__device__ __forceinline__ float xor32_add(float x) { auto r = __builtin_amdgcn_permlane32_swap(__float_as_uint(x), __float_as_uint(x), false, false); return __uint_as_float(r[0]) + __uint_as_float(r[1]); }
__device__ __forceinline__ float xor16_add(float x) { auto r = __builtin_amdgcn_permlane16_swap(__float_as_uint(x), __float_as_uint(x), false, false); return __uint_as_float(r[0]) + __uint_as_float(r[1]); }
__device__ __forceinline__ float ror8(float x) { return __int_as_float(__builtin_amdgcn_update_dpp(0, __float_as_int(x), 0x128, 0xf, 0xf, false)); }

typedef __attribute__((ext_vector_type(2))) float f32x2;
__device__ __forceinline__ unsigned cvtpk(float a, float b) { unsigned r; asm("v_cvt_pk_bf16_f32 %0, %1, %2" : "=v"(r) : "v"(a), "v"(b)); return r; }
__device__ __forceinline__ float vmax(float a, float b) { float r; asm("v_max_f32 %0, %1, %2" : "=v"(r) : "v"(a), "v"(b)); return r; }
__device__ __forceinline__ float vmax3(float a, float b, float c) { float r; asm("v_max3_f32 %0, %1, %2, %3" : "=v"(r) : "v"(a), "v"(b), "v"(c)); return r; }
__device__ __forceinline__ float xmax32(float x) { auto r = __builtin_amdgcn_permlane32_swap(__float_as_uint(x), __float_as_uint(x), false, false); return vmax(__uint_as_float(r[0]), __uint_as_float(r[1])); }
__device__ __forceinline__ float xmax16(float x) { auto r = __builtin_amdgcn_permlane16_swap(__float_as_uint(x), __float_as_uint(x), false, false); return vmax(__uint_as_float(r[0]), __uint_as_float(r[1])); }
constexpr unsigned KM_BYTES = (unsigned)NSEG * NH * MT * DH * 2;
__device__ __forceinline__ void xattn_unit(__amdgpu_buffer_rsrc_t qrs, __amdgpu_buffer_rsrc_t krs, bf16x8* __restrict__ Oft, int s, int h, int lane) {
    const int g = lane >> 4, i = lane & 15;
    const unsigned qo = (unsigned)((s * NH + h) * 8192 + lane * 16), mo = (unsigned)((s * NH + h) * 2048);
#define LDQ(off) __builtin_bit_cast(bf16x8, __builtin_amdgcn_raw_buffer_load_b128(qrs, (off), 0, 0))
    bf16x8 kf[4], k1[4], k2[4], qf[2][4], vx[8]; v2u vm[8];
#pragma unroll
    for (int ks = 0; ks < 4; ++ks) {
        kf[ks] = __builtin_bit_cast(bf16x8, __builtin_amdgcn_raw_buffer_load_b128(krs, mo + (unsigned)((i & 7) * 256 + 64 * ks + 16 * g), 0, 0));
        k1[ks] = LDQ(QKV_MAT_BYTES + qo + ks * 1024);
        k2[ks] = LDQ(QKV_MAT_BYTES + qo + (4 + ks) * 1024);
        qf[0][ks] = LDQ(qo + ks * 1024);
        qf[1][ks] = LDQ(qo + (4 + ks) * 1024);
    }
#pragma unroll
    for (int tau = 0; tau < 8; ++tau) {
        vx[tau] = LDQ(2u * QKV_MAT_BYTES + qo + tau * 1024);
        vm[tau] = __builtin_amdgcn_raw_buffer_load_b64(krs, KM_BYTES + mo + (unsigned)((tau * 32 + (lane & 31)) * 8), 0, 0);
    }
#pragma unroll
    for (int qt = 0; qt < 2; ++qt) {
        f32x4 sc[3];
#pragma unroll
        for (int a = 0; a < 3; ++a) sc[a] = (f32x4){0.f, 0.f, 0.f, 0.f};
#pragma unroll
        for (int ks = 0; ks < 4; ++ks) {
            sc[0] = MFMA(kf[ks], qf[qt][ks], sc[0]);
            sc[1] = MFMA(k1[ks], qf[qt][ks], sc[1]);
            sc[2] = MFMA(k2[ks], qf[qt][ks], sc[2]);
        }
        const int tq = qt * 16 + i;
        float sv[12]; bool ok[12];
#pragma unroll
        for (int r = 0; r < 4; ++r) { ok[r] = g < 2; ok[4 + r] = (8 * g + r) <= tq; ok[8 + r] = (8 * g + 4 + r) <= tq; }
        float mx = -3.0e38f;
#pragma unroll
        for (int a = 0; a < 3; ++a)
#pragma unroll
            for (int r = 0; r < 4; ++r) { sv[a * 4 + r] = rbf(rbf(sc[a][r]) * SCALE); mx = ok[a * 4 + r] ? fmaxf(mx, sv[a * 4 + r]) : mx; }
        mx = xor16_max(mx);
        mx = xor32_max(mx);
        float ex[12], sum = 0.f;
#pragma unroll
        for (int a = 0; a < 12; ++a) { ex[a] = ok[a] ? __expf(sv[a] - mx) : 0.f; sum += ex[a]; }
        sum = xor16_add(sum);
        sum = xor32_add(sum);
        const float inv = 1.0f / sum;
        const bf16x8 pX = mk8(pack2(ex[4] * inv, ex[5] * inv), pack2(ex[6] * inv, ex[7] * inv), pack2(ex[8] * inv, ex[9] * inv), pack2(ex[10] * inv, ex[11] * inv));
        const bf16x8 pM = mk8(pack2(ex[0] * inv, ex[1] * inv), pack2(ex[2] * inv, ex[3] * inv), 0u, 0u);
#pragma unroll
        for (int ks = 0; ks < 4; ++ks) {
            f32x4 o[2];
#pragma unroll
            for (int t = 0; t < 2; ++t) {
                const int tau = 2 * ks + t;
                o[t] = MFMA(vx[tau], pX, ((f32x4){0.f, 0.f, 0.f, 0.f}));
                o[t] = MFMA(mk8(vm[tau].x, vm[tau].y, 0u, 0u), pM, o[t]);
            }
            Oft[((size_t)(s * 2 + qt) * 128 + h * 4 + ks) * 64 + lane] =
                mk8(pack2(o[0][0], o[0][1]), pack2(o[0][2], o[0][3]), pack2(o[1][0], o[1][1]), pack2(o[1][2], o[1][3]));
        }
    }
}

__global__ __launch_bounds__(256) void k_xattn(u16* QX, u16* KM, bf16x8* __restrict__ Oft) {
    const __amdgpu_buffer_rsrc_t qrs = __builtin_amdgcn_make_buffer_rsrc(QX, 0, 3u * QKV_MAT_BYTES, 0x00020000);
    const __amdgpu_buffer_rsrc_t krs = __builtin_amdgcn_make_buffer_rsrc(KM, 0, 2u * KM_BYTES, 0x00020000);
    const int wid = blockIdx.x * 4 + __builtin_amdgcn_readfirstlane(threadIdx.x >> 6);
    xattn_unit(qrs, krs, Oft, wid >> 5, wid & 31, threadIdx.x & 63);
}

#define AMFMA0(d, a, b) asm volatile("s_nop 1\n\tv_mfma_f32_16x16x32_bf16 %0, %1, %2, 0" : "=&v"(d) : "v"(a), "v"(b))
#define AMFMA(d, a, b) asm volatile("s_nop 1\n\tv_mfma_f32_16x16x32_bf16 %0, %1, %2, %0" : "+v"(d) : "v"(a), "v"(b))
#define AMFMA16(d, a, b) asm volatile("s_nop 1\n\tv_mfma_f32_16x16x16_bf16 %0, %1, %2, %0" : "+v"(d) : "v"(a), "v"(b))

__global__ __launch_bounds__(256, 1) void k_chain(const u16* __restrict__ mem0, const bf16x8* __restrict__ WT, const bf16x8* __restrict__ WOT,
                                                  u16* __restrict__ KM, u16* __restrict__ VM, unsigned* Pbuf, unsigned* Mbuf, unsigned* tmo,
                                                  const bf16x8* __restrict__ XN, const bf16x8* __restrict__ WG, const bf16x8* __restrict__ WU,
                                                  const bf16x8* __restrict__ WD, u16* __restrict__ QX, u16* __restrict__ Bout) {
    extern __shared__ __attribute__((aligned(16))) char lds[];
    const int h = blockIdx.x;
    const int w = __builtin_amdgcn_readfirstlane(threadIdx.x >> 6), lane = threadIdx.x & 63, g = lane >> 4, i = lane & 15;
    const __amdgpu_buffer_rsrc_t qrs = __builtin_amdgcn_make_buffer_rsrc(QX, 0, 3u * QKV_MAT_BYTES, 0x00020000);
    const __amdgpu_buffer_rsrc_t krs = __builtin_amdgcn_make_buffer_rsrc(KM, 0, 2u * KM_BYTES, 0x00020000);
    unsigned* kvcnt = tmo + 16;
    if (blockIdx.x >= NH) {
        const int wb = blockIdx.x - NH;
        for (int t = wb; t < 1024; t += NWORK) {
            qkv_tile(lds, XN, WT, qrs, t >> 6, 1 + ((t >> 5) & 1), t & 31, w, lane);
            asm volatile("s_waitcnt vmcnt(0)" ::: "memory");
            __syncthreads();
            if (threadIdx.x == 0) __hip_atomic_fetch_add(kvcnt + (t >> 6), 1u, __ATOMIC_RELAXED, __HIP_MEMORY_SCOPE_AGENT);
        }
        __syncthreads();
        if (wb < 64) {
            glu_block(XN, WG, WU, WD, Bout, lds, wb, w, lane);
        } else {
            for (int t = wb - 64; t < 512; t += NWORK - 64) {
                qkv_tile(lds, XN, WT, qrs, t >> 5, 0, t & 31, w, lane);
            }
        }
        return;
    }
    unsigned kv_ready = 0;
    auto wait_kv = [&](int grp) {
        unsigned spins = 0;
        while (!((kv_ready >> grp) & 1u)) {
            const unsigned c = __hip_atomic_load(kvcnt + (lane & 15), __ATOMIC_RELAXED, __HIP_MEMORY_SCOPE_AGENT);
            kv_ready = (unsigned)__ballot(c >= 64u) & 0xffffu;
            if ((kv_ready >> grp) & 1u) break;
            __builtin_amdgcn_s_sleep(2);
            if (++spins > SPINCAP) { if (lane == 0) atomicAdd(tmo, 1u); break; }
        }
    };
    auto fetch_kv = [&](int sg, v4u (&pk)[2], v4u (&pv)[2]) {
        const unsigned o = (unsigned)((sg * NH + h) * 8192 + lane * 16);
        pk[0] = __builtin_amdgcn_raw_buffer_load_b128(qrs, QKV_MAT_BYTES + o + w * 1024, 0, 16);
        pk[1] = __builtin_amdgcn_raw_buffer_load_b128(qrs, QKV_MAT_BYTES + o + w * 1024 + 4096, 0, 16);
        pv[0] = __builtin_amdgcn_raw_buffer_load_b128(qrs, 2u * QKV_MAT_BYTES + o + w * 2048, 0, 16);
        pv[1] = __builtin_amdgcn_raw_buffer_load_b128(qrs, 2u * QKV_MAT_BYTES + o + w * 2048 + 1024, 0, 16);
    };
    const __amdgpu_buffer_rsrc_t prs = __builtin_amdgcn_make_buffer_rsrc(Pbuf, 0, 63 * 32 * 16384, 0x00020000);
    const __amdgpu_buffer_rsrc_t mrs = __builtin_amdgcn_make_buffer_rsrc(Mbuf, 0, 64 * 8192, 0x00020000);

    bf16x8 WA[64], WV[32];
    {
        const bf16x8* src = WT + (size_t)(h * 4 + w) * 96 * 64 + lane;
#pragma unroll
        for (int f = 0; f < 64; ++f) WA[f] = src[f * 64];
#pragma unroll
        for (int f = 0; f < 32; ++f) WV[f] = src[(64 + f) * 64];
        const bf16x8* so = WOT + ((size_t)h * 128 + w * 32) * 64 + lane;
        bf16x8* lo = (bf16x8*)(lds + L_WO) + (w * 32) * 64 + lane;
#pragma unroll 8
        for (int f = 0; f < 32; ++f) lo[f * 64] = so[f * 64];
    }
    {
        const int tid = threadIdx.x;
#pragma unroll
        for (int c = tid; c < 512; c += 256) {
            const int t = c & 7, gg = (c >> 3) & 3, kk = c >> 5;
            *(v4u*)(lds + L_MEM + c * 16) = *(const v4u*)(mem0 + t * DM + 32 * kk + 8 * gg);
        }
    }
    v4u pk[2], pv[2];
    wait_kv(0);
    fetch_kv(0, pk, pv);
    const int red = h * 4 + w;
#define STAMP(k)
    for (int s = 0; s < NSEG; ++s) {
        __syncthreads();
        STAMP(0);
        asm volatile("" : "+v"(pk[0]), "+v"(pk[1]), "+v"(pv[0]), "+v"(pv[1]));
        f32x4 acc[6];
        bf16x8 mf[16];
        const char* mp = lds + L_MEM + g * 128 + (i & 7) * 16;
        {
#pragma unroll
            for (int kk = 0; kk < 4; ++kk) mf[kk] = *(const bf16x8*)(mp + kk * 512);
#pragma unroll
            for (int kk = 0; kk < 16; ++kk) {
                if (kk + 4 < 16) mf[kk + 4] = *(const bf16x8*)(mp + (kk + 4) * 512);
                if (kk == 0) {
                    asm volatile("v_mfma_f32_16x16x32_bf16 %0, %4, %8, 0\n\tv_mfma_f32_16x16x32_bf16 %1, %5, %8, 0\n\t"
                                 "v_mfma_f32_16x16x32_bf16 %2, %6, %8, 0\n\tv_mfma_f32_16x16x32_bf16 %3, %7, %8, 0"
                                 : "=&v"(acc[0]), "=&v"(acc[1]), "=&v"(acc[2]), "=&v"(acc[3])
                                 : "a"(WA[0]), "a"(WA[16]), "a"(WA[32]), "a"(WA[48]), "v"(mf[0]));
                } else {
                    asm volatile("v_mfma_f32_16x16x32_bf16 %0, %4, %8, %0\n\tv_mfma_f32_16x16x32_bf16 %1, %5, %8, %1\n\t"
                                 "v_mfma_f32_16x16x32_bf16 %2, %6, %8, %2\n\tv_mfma_f32_16x16x32_bf16 %3, %7, %8, %3"
                                 : "+v"(acc[0]), "+v"(acc[1]), "+v"(acc[2]), "+v"(acc[3])
                                 : "a"(WA[kk]), "a"(WA[16 + kk]), "a"(WA[32 + kk]), "a"(WA[48 + kk]), "v"(mf[kk]));
                }
            }
            asm volatile("s_nop 7\n\ts_nop 1" : "+v"(acc[0]), "+v"(acc[1]), "+v"(acc[2]), "+v"(acc[3]));
        }
        const bf16x8 qf = mk8(pack2(acc[0][0], acc[0][1]), pack2(acc[0][2], acc[0][3]), pack2(acc[1][0], acc[1][1]), pack2(acc[1][2], acc[1][3]));
        const bf16x8 kf = mk8(pack2(acc[2][0], acc[2][1]), pack2(acc[2][2], acc[2][3]), pack2(acc[3][0], acc[3][1]), pack2(acc[3][2], acc[3][3]));
        if (i < 8) __builtin_amdgcn_raw_buffer_store_b128(__builtin_bit_cast(v4u, kf), krs, (unsigned)((s * NH + h) * 2048 + i * 256 + 64 * w + 16 * g), 0, 0);
        {
            f32x4 sp[3];
            AMFMA0(sp[0], kf, qf); AMFMA0(sp[1], pk[0], qf); AMFMA0(sp[2], pk[1], qf);
            asm volatile("s_nop 7" : "+v"(sp[0]), "+v"(sp[1]), "+v"(sp[2]));
            if (i < 8) {
#pragma unroll
                for (int a = 0; a < 3; ++a) *(f32x4*)(lds + L_SC + (((w * 3 + a) * 4 + g) * 8 + i) * 16) = sp[a];
            }
        }
#pragma unroll
        for (int kk = 0; kk < 4; ++kk) mf[kk] = *(const bf16x8*)(mp + kk * 512);
        STAMP(1);
        __syncthreads();
        STAMP(2);
#define VS(kk, hf)                                                                                                             \
        do {                                                                                                                   \
            if ((hf) == 0 && (kk) + 4 < 16) mf[((kk) + 4) & 15] = *(const bf16x8*)(mp + ((kk) + 4) * 512);                     \
            if ((kk) == 0) asm volatile("v_mfma_f32_16x16x32_bf16 %0, %2, %1, 0" : "=&v"(acc[4 + (hf)]) : "v"(WV[16 * (hf)]), "v"(mf[0]));      \
            else asm volatile("v_mfma_f32_16x16x32_bf16 %0, %2, %1, %0" : "+v"(acc[4 + (hf)]) : "v"(WV[16 * (hf) + (kk)]), "v"(mf[kk]));        \
        } while (0)
        v2u v0, v1;
        {
            f32x4 pp[4][3];
#pragma unroll
            for (int ww = 0; ww < 4; ++ww)
#pragma unroll
                for (int a = 0; a < 3; ++a) pp[ww][a] = *(const f32x4*)(lds + L_SC + (((ww * 3 + a) * 4 + g) * 8 + (i & 7)) * 16);
            VS(0, 0); VS(0, 1); VS(1, 0); VS(1, 1); VS(2, 0); VS(2, 1); VS(3, 0); VS(3, 1);
            float sv[12];
#define SCHUNK(a, k0)                                                                                                          \
            do {                                                                                                               \
                const f32x4 t0_ = pp[0][a] + pp[1][a];                                                                         \
                VS(k0, 0);                                                                                                     \
                const f32x4 t_ = t0_ + (pp[2][a] + pp[3][a]);                                                                  \
                VS(k0, 1);                                                                                                     \
                const unsigned u0_ = cvtpk(t_[0], t_[1]), u1_ = cvtpk(t_[2], t_[3]);                                           \
                const f32x4 b_ = (f32x4){__uint_as_float(u0_ << 16), __uint_as_float(u0_ & 0xffff0000u), __uint_as_float(u1_ << 16),    \
                                         __uint_as_float(u1_ & 0xffff0000u)} * SCALE;                                          \
                VS((k0) + 1, 0);                                                                                               \
                const unsigned w0_ = cvtpk(b_[0], b_[1]), w1_ = cvtpk(b_[2], b_[3]);                                           \
                sv[(a) * 4 + 0] = __uint_as_float(w0_ << 16); sv[(a) * 4 + 1] = __uint_as_float(w0_ & 0xffff0000u);           \
                sv[(a) * 4 + 2] = __uint_as_float(w1_ << 16); sv[(a) * 4 + 3] = __uint_as_float(w1_ & 0xffff0000u);           \
                VS((k0) + 1, 1);                                                                                               \
            } while (0)
            SCHUNK(0, 4); SCHUNK(1, 6); SCHUNK(2, 8);
            const bool mv = g < 2;
            float mx = vmax3(vmax3(sv[4], sv[5], sv[6]), vmax3(sv[7], sv[8], sv[9]), vmax(sv[10], sv[11]));
            VS(10, 0);
            const float mxm = vmax3(vmax3(sv[0], sv[1], sv[2]), sv[3], mx);
            mx = mv ? mxm : mx;
            VS(10, 1);
            mx = xmax16(mx);
            VS(11, 0);
            mx = xmax32(mx);
            VS(11, 1);
            constexpr float L2E = 1.4426950408889634f;
            const float nm = -mx * L2E;
            float ex[12];
#pragma unroll
            for (int a = 0; a < 4; ++a) { ex[a] = __builtin_amdgcn_exp2f(__builtin_fmaf(sv[a], L2E, nm)); ex[a] = mv ? ex[a] : 0.f; if (a & 1) VS(12, a >> 1); }
#pragma unroll
            for (int a = 4; a < 8; ++a) { ex[a] = __builtin_amdgcn_exp2f(__builtin_fmaf(sv[a], L2E, nm)); if (a & 1) VS(13, (a >> 1) & 1); }
#pragma unroll
            for (int a = 8; a < 12; ++a) { ex[a] = __builtin_amdgcn_exp2f(__builtin_fmaf(sv[a], L2E, nm)); if (a & 1) VS(14, (a >> 1) & 1); }
            float sum = (((ex[0] + ex[1]) + (ex[2] + ex[3])) + ((ex[4] + ex[5]) + (ex[6] + ex[7])));
            VS(15, 0);
            sum += ((ex[8] + ex[9]) + (ex[10] + ex[11]));
            VS(15, 1);
            sum = xor16_add(sum);
            sum = xor32_add(sum);
            const float inv = __builtin_amdgcn_rcpf(sum);
            const bf16x8 pX = mk8(cvtpk(ex[4] * inv, ex[5] * inv), cvtpk(ex[6] * inv, ex[7] * inv), cvtpk(ex[8] * inv, ex[9] * inv), cvtpk(ex[10] * inv, ex[11] * inv));
            const v2u pM = {cvtpk(ex[0] * inv, ex[1] * inv), cvtpk(ex[2] * inv, ex[3] * inv)};
            f32x4 o0, o1;
            AMFMA0(o0, pv[0], pX); AMFMA0(o1, pv[1], pX);
            asm volatile("s_nop 3" : "+v"(acc[4]), "+v"(acc[5]));
            v0 = (v2u){cvtpk(acc[4][0], acc[4][1]), cvtpk(acc[4][2], acc[4][3])};
            v1 = (v2u){cvtpk(acc[5][0], acc[5][1]), cvtpk(acc[5][2], acc[5][3])};
            AMFMA16(o0, v0, pM); AMFMA16(o1, v1, pM);
            asm volatile("s_nop 7" : "+v"(o0), "+v"(o1));
            if (i < 8) *(bf16x8*)(lds + L_OE + ((w * 4 + g) * 8 + i) * 16) = mk8(cvtpk(o0[0], o0[1]), cvtpk(o0[2], o0[3]), cvtpk(o1[0], o1[1]), cvtpk(o1[2], o1[3]));
        }
        if (g < 2) {
            __builtin_amdgcn_raw_buffer_store_b64(v0, krs, KM_BYTES + (unsigned)((s * NH + h) * 2048 + (2 * w * 32 + lane) * 8), 0, 0);
            __builtin_amdgcn_raw_buffer_store_b64(v1, krs, KM_BYTES + (unsigned)((s * NH + h) * 2048 + ((2 * w + 1) * 32 + lane) * 8), 0, 0);
        }
        if (s == NSEG - 1) break;
        const unsigned pofs = (unsigned)s * (32u * 16384u);
        {
            const char* wp = lds + L_WO + (w * 32) * 1024 + lane * 16;
            bf16x8 wf[4][8];
#pragma unroll
            for (int f = 0; f < 8; ++f) wf[0][f] = *(const bf16x8*)(wp + f * 1024);
            __syncthreads();
            STAMP(3);
            bf16x8 of[4];
#pragma unroll
            for (int ks = 0; ks < 4; ++ks) of[ks] = *(const bf16x8*)(lds + L_OE + ((ks * 4 + g) * 8 + (i & 7)) * 16);
#pragma unroll
            for (int a = 0; a < 4; ++a) {
                if (a < 3) {
#pragma unroll
                    for (int f = 0; f < 8; ++f) wf[a + 1][f] = *(const bf16x8*)(wp + ((a + 1) * 8 + f) * 1024);
                }
                f32x4 d0, d1;
                AMFMA0(d0, wf[a][0], of[0]); AMFMA0(d1, wf[a][4], of[0]);
#pragma unroll
                for (int ks = 1; ks < 4; ++ks) { AMFMA(d0, wf[a][ks], of[ks]); AMFMA(d1, wf[a][4 + ks], of[ks]); }
                asm volatile("s_nop 7" : "+v"(d0), "+v"(d1));
                v4u v;
#pragma unroll
                for (int r = 0; r < 4; ++r) {
                    const float x1 = ror8(d1[r]);
                    v[r] = __float_as_uint(i < 8 ? d0[r] : x1) | 1u;
                }
                const int T = 8 * w + 2 * a + (i >> 3);
                __builtin_amdgcn_raw_buffer_store_b128(v, prs, pofs + (unsigned)(h * 16384 + (4 * T + g) * 128 + (i & 7) * 16), 0, 16);
            }
        }
        STAMP(4);
        if (((s + 1) & 3) == 0) wait_kv((s + 1) >> 2);
        fetch_kv(s + 1, pk, pv);
        for (int z = 0; z < SLP1; ++z) __builtin_amdgcn_s_sleep(1);
        {
            v4u r[4];
            unsigned spins = 0;
            for (;;) {
#pragma unroll
                for (int q = 0; q < 4; ++q)
                    r[q] = __builtin_amdgcn_raw_buffer_load_b128(prs, pofs + (unsigned)((q * 8 + (lane >> 3)) * 16384 + red * 128 + (lane & 7) * 16), 0, 16);
                bool ok = true;
#pragma unroll
                for (int q = 0; q < 4; ++q) ok &= (r[q].x & r[q].y & r[q].z & r[q].w & 1u) != 0u;
                if (__all(ok)) break;
                if (++spins > SPINCAP) { if (lane == 0) atomicAdd(tmo, 1u); break; }
            }
            STAMP(5);
            float sm[4];
#pragma unroll
            for (int c = 0; c < 4; ++c) {
                sm[c] = (__uint_as_float(r[0][c] & ~1u) + __uint_as_float(r[1][c] & ~1u)) + (__uint_as_float(r[2][c] & ~1u) + __uint_as_float(r[3][c] & ~1u));
                sm[c] += ror8(sm[c]);
                sm[c] = xor16_add(sm[c]);
                sm[c] = xor32_add(sm[c]);
            }
            if (lane < 8) {
                v2u v = {pack2(sm[0], sm[1]), pack2(sm[2], sm[3])};
                if (v.x == 0u) v.x = 0x8000u;
                if (v.y == 0u) v.y = 0x8000u;
                __builtin_amdgcn_raw_buffer_store_b64(v, mrs, (unsigned)(s * 8192 + red * 64 + lane * 8), 0, 16);
            }
        }
        STAMP(6);
        for (int z = 0; z < SLP2; ++z) __builtin_amdgcn_s_sleep(1);
        {
            v4u m[2];
            unsigned spins = 0;
            for (;;) {
#pragma unroll
                for (int q = 0; q < 2; ++q) m[q] = __builtin_amdgcn_raw_buffer_load_b128(mrs, (unsigned)(s * 8192 + w * 2048 + q * 1024 + lane * 16), 0, 16);
                bool ok = true;
#pragma unroll
                for (int q = 0; q < 2; ++q) ok &= (m[q].x != 0u) & (m[q].y != 0u) & (m[q].z != 0u) & (m[q].w != 0u);
                if (__all(ok)) break;
                if (++spins > SPINCAP) { if (lane == 0) atomicAdd(tmo, 1u); break; }
            }
#pragma unroll
            for (int q = 0; q < 2; ++q) {
                const int R = w * 32 + q * 16 + (lane >> 2), t = 2 * (lane & 3);
                char* dst = lds + L_MEM + (R >> 3) * 512 + ((R >> 1) & 3) * 128 + t * 16 + (R & 1) * 8;
                *(v2u*)dst = (v2u){m[q].x, m[q].y};
                *(v2u*)(dst + 16) = (v2u){m[q].z, m[q].w};
            }
        }
        STAMP(7);
    }
}

__global__ __launch_bounds__(256, 1) void k_final(const bf16x8* __restrict__ Oft, const bf16x8* __restrict__ WOT, f32x4* __restrict__ part) {
    __shared__ bf16x8 sm[4][32][64];
    const int w = __builtin_amdgcn_readfirstlane(threadIdx.x >> 6), lane = threadIdx.x & 63;
    const int xcd = blockIdx.x & 7, j = blockIdx.x >> 3, mtile = xcd * 2 + (j >> 4), ntile = (j >> 2) & 3, kq = j & 3;
    const int wm = w >> 1, wn = w & 1;
    const bf16x8* sp = w < 2 ? Oft + ((size_t)(mtile * 8 + w * 4) * 128 + kq * 32) * 64 + lane
                             : WOT + (((size_t)(kq * 8) * 32 + ntile * 8 + (w - 2) * 4) * 4) * 64 + lane;
    auto stage = [&](int c, int buf) {
#pragma unroll
        for (int f = 0; f < 8; ++f) {
            const int t = f >> 1, kk = 2 * c + (f & 1);
            const int off = w < 2 ? (t * 128 + kk) * 64 : (((kk >> 2) * 32 + t) * 4 + (kk & 3)) * 64;
            GLDS16(sp + off, &sm[buf][w * 8 + f][0]);
        }
    };
    f32x4 acc[4][4];
#pragma unroll
    for (int a = 0; a < 4; ++a)
#pragma unroll
        for (int b = 0; b < 4; ++b) acc[a][b] = (f32x4){0.f, 0.f, 0.f, 0.f};
    stage(0, 0); stage(1, 1); stage(2, 2);
#pragma unroll
    for (int c = 0; c < 16; ++c) {
        if (c < 14) asm volatile("s_waitcnt vmcnt(16)\n\ts_barrier" ::: "memory");
        else if (c == 14) asm volatile("s_waitcnt vmcnt(8)\n\ts_barrier" ::: "memory");
        else asm volatile("s_waitcnt vmcnt(0)\n\ts_barrier" ::: "memory");
        if (c + 3 < 16) stage(c + 3, (c + 3) & 3);
#pragma unroll
        for (int k2 = 0; k2 < 2; ++k2) {
            bf16x8 a[4], b[4];
#pragma unroll
            for (int t = 0; t < 4; ++t) { a[t] = sm[c & 3][(wm * 4 + t) * 2 + k2][lane]; b[t] = sm[c & 3][16 + (wn * 4 + t) * 2 + k2][lane]; }
#pragma unroll
            for (int mt = 0; mt < 4; ++mt)
#pragma unroll
                for (int t = 0; t < 4; ++t) acc[mt][t] = MFMA(b[t], a[mt], acc[mt][t]);
        }
    }
#pragma unroll
    for (int mt = 0; mt < 4; ++mt)
#pragma unroll
        for (int t = 0; t < 4; ++t)
            part[(((size_t)kq * 128 + mtile * 8 + wm * 4 + mt) * 32 + ntile * 8 + wn * 4 + t) * 64 + lane] = acc[mt][t];
}

__global__ __launch_bounds__(256) void k_out(const f32x4* __restrict__ part, const u16* __restrict__ Bout, const u16* __restrict__ x,
                                             u16* __restrict__ out) {
    const int id = blockIdx.x * 256 + threadIdx.x, lane = id & 63, tile = id >> 6, g = lane >> 4, i = lane & 15;
    constexpr size_t KS = (size_t)128 * 32 * 64;
    const f32x4 a = (part[id] + part[KS + id]) + (part[2 * KS + id] + part[3 * KS + id]);
    const size_t idx = (size_t)((tile >> 5) * 16 + i) * DM + (tile & 31) * 16 + 4 * g;
    const v2u xr = *(const v2u*)(x + idx), br = *(const v2u*)(Bout + idx);
    const float xv[4] = {__uint_as_float(xr.x << 16), __uint_as_float(xr.x & 0xffff0000u), __uint_as_float(xr.y << 16), __uint_as_float(xr.y & 0xffff0000u)};
    const float bv[4] = {__uint_as_float(br.x << 16), __uint_as_float(br.x & 0xffff0000u), __uint_as_float(br.y << 16), __uint_as_float(br.y & 0xffff0000u)};
    float y[4];
#pragma unroll
    for (int r = 0; r < 4; ++r) y[r] = rbf(xv[r] + rbf(a[r])) + bv[r];
    *(v2u*)(out + idx) = (v2u){pack2(y[0], y[1]), pack2(y[2], y[3])};
}

extern "C" void kernel_launch(void* const* d_in, const int* in_sizes, int n_in, void* d_out, int out_size, void* d_ws, size_t ws_size,
                              hipStream_t stream) {
    const u16* x = (const u16*)d_in[0];
    const u16* mem0 = (const u16*)d_in[1];
    const u16* Wq = (const u16*)d_in[2];
    const u16* Wk = (const u16*)d_in[3];
    const u16* Wv = (const u16*)d_in[4];
    const u16* Wo = (const u16*)d_in[5];
    const u16* Wg = (const u16*)d_in[8];
    const u16* Wu = (const u16*)d_in[9];
    const u16* Wd = (const u16*)d_in[10];
    char* ws = (char*)d_ws;
    (void)hipFuncSetAttribute((const void*)k_chain, hipFuncAttributeMaxDynamicSharedMemorySize, L_TOTAL);
    k_prep<<<PREP_LN_BLOCKS + PREP_TILE_BLOCKS + PREP_ZERO_BLOCKS, 256, 0, stream>>>(x, Wq, Wk, Wv, Wo, Wg, Wu, Wd, ws);
    k_chain<<<NH + NWORK, 256, L_TOTAL, stream>>>(mem0, (const bf16x8*)(ws + OFF_WT), (const bf16x8*)(ws + OFF_WOT), (u16*)(ws + OFF_KM), (u16*)(ws + OFF_VM), (unsigned*)(ws + OFF_P),
                                                  (unsigned*)(ws + OFF_M), (unsigned*)(ws + OFF_TMO), (const bf16x8*)(ws + OFF_XN),
                                                  (const bf16x8*)(ws + OFF_WG), (const bf16x8*)(ws + OFF_WU), (const bf16x8*)(ws + OFF_WD),
                                                  (u16*)(ws + OFF_QX), (u16*)(ws + OFF_H));
    k_xattn<<<NSEG * NH / 4, 256, 0, stream>>>((u16*)(ws + OFF_QX), (u16*)(ws + OFF_KM), (bf16x8*)(ws + OFF_O));
    k_final<<<256, 256, 0, stream>>>((const bf16x8*)(ws + OFF_O), (const bf16x8*)(ws + OFF_WOT), (f32x4*)(ws + OFF_PART));
    k_out<<<S_ * DM / 4 / 256, 256, 0, stream>>>((const f32x4*)(ws + OFF_PART), (const u16*)(ws + OFF_H), x, (u16*)d_out);
}
```

```cpp
#include <hip/hip_runtime.h>
#include <hip/hip_bf16.h>

constexpr int S_ = 2048, DM = 512, NH = 32, DH = 128, MT = 8, SEG = 32, NSEG = 64, FF = 1792;

typedef unsigned short u16;
typedef __attribute__((ext_vector_type(8))) short bf16x8;
typedef __attribute__((ext_vector_type(4))) float f32x4;
typedef __attribute__((ext_vector_type(4))) unsigned v4u;
typedef __attribute__((ext_vector_type(2))) unsigned v2u;
typedef __bf16 bf16x2_t __attribute__((ext_vector_type(2)));

__device__ __forceinline__ unsigned pack2(float a, float b) {
    bf16x2_t v = {(__bf16)a, (__bf16)b};
    return __builtin_bit_cast(unsigned, v);
}
__device__ __forceinline__ float rbf(float a) { return (float)(__bf16)a; }
__device__ __forceinline__ float bf2f(u16 b) { return __uint_as_float(((unsigned)b) << 16); }
__device__ __forceinline__ bf16x8 mk8(unsigned a, unsigned b, unsigned c, unsigned d) {
    v4u v = {a, b, c, d};
    return __builtin_bit_cast(bf16x8, v);
}
#define MFMA(a, b, c) __builtin_amdgcn_mfma_f32_16x16x32_bf16((a), (b), (c), 0, 0, 0)

__device__ __forceinline__ int pi_col(int tau, int rho) { return 32 * (tau >> 1) + 8 * (rho >> 2) + 4 * (tau & 1) + (rho & 3); }

constexpr size_t OFF_TMO = 0;
constexpr size_t OFF_M   = 65536;
constexpr size_t OFF_P   = OFF_M + 64 * 8192;
constexpr size_t ZERO_BYTES = OFF_P + (size_t)63 * 32 * 16384;
constexpr size_t OFF_XN  = (ZERO_BYTES + 4095) / 4096 * 4096;
constexpr size_t OFF_WT  = OFF_XN + (size_t)S_ * DM * 2;
constexpr size_t OFF_WOT = OFF_WT + (size_t)3 * DM * NH * DH * 2;
constexpr size_t OFF_WG  = OFF_WOT + (size_t)DM * NH * DH * 2;
constexpr size_t OFF_WU  = OFF_WG + (size_t)DM * FF * 2;
constexpr size_t OFF_WD  = OFF_WU + (size_t)DM * FF * 2;
constexpr size_t OFF_QX  = OFF_WD + (size_t)DM * FF * 2;
constexpr size_t OFF_KX  = OFF_QX + (size_t)S_ * NH * DH * 2;
constexpr size_t OFF_VXT = OFF_KX + (size_t)S_ * NH * DH * 2;
constexpr size_t OFF_KM  = OFF_VXT + (size_t)S_ * NH * DH * 2;
constexpr size_t OFF_VM  = OFF_KM + (size_t)NSEG * NH * MT * DH * 2;
constexpr size_t OFF_H   = OFF_VM + (size_t)NSEG * NH * MT * DH * 2;
constexpr size_t OFF_O   = OFF_H + (size_t)S_ * FF * 2;
constexpr size_t OFF_PART = OFF_O + (size_t)S_ * NH * DH * 2;
constexpr size_t WS_TOTAL = OFF_PART + (size_t)4 * S_ * DM * 4;

__device__ __forceinline__ void ln_row(const u16* __restrict__ x, u16* __restrict__ xn_ft, int row, int lane) {
    const v4u raw = *(const v4u*)(x + (size_t)row * DM + lane * 8);
    float v[8];
    v[0] = __uint_as_float(raw.x << 16); v[1] = __uint_as_float(raw.x & 0xffff0000u);
    v[2] = __uint_as_float(raw.y << 16); v[3] = __uint_as_float(raw.y & 0xffff0000u);
    v[4] = __uint_as_float(raw.z << 16); v[5] = __uint_as_float(raw.z & 0xffff0000u);
    v[6] = __uint_as_float(raw.w << 16); v[7] = __uint_as_float(raw.w & 0xffff0000u);
    float s = 0.f;
#pragma unroll
    for (int j = 0; j < 8; ++j) s += v[j];
#pragma unroll
    for (int o = 1; o < 64; o <<= 1) s += __shfl_xor(s, o);
    const float mean = s * (1.0f / DM);
    float q = 0.f;
#pragma unroll
    for (int j = 0; j < 8; ++j) { const float d = v[j] - mean; q += d * d; }
#pragma unroll
    for (int o = 1; o < 64; o <<= 1) q += __shfl_xor(q, o);
    const float mb = rbf(mean), vb = rbf(q * (1.0f / DM));
    const float den = rbf(sqrtf(rbf(vb + 1e-05f)));
    float y[8];
#pragma unroll
    for (int j = 0; j < 8; ++j) y[j] = rbf(v[j] - mb) / den;
    v4u o4 = {pack2(y[0], y[1]), pack2(y[2], y[3]), pack2(y[4], y[5]), pack2(y[6], y[7])};
    const int mt = row >> 4, kk = lane >> 2, fl = (lane & 3) * 16 + (row & 15);
    *(v4u*)(xn_ft + (((size_t)mt * 16 + kk) * 64 + fl) * 8) = o4;
}

__device__ __forceinline__ void transpose8x8(const v4u (&r)[8], v4u (&o)[8]) {
#pragma unroll
    for (int c = 0; c < 4; ++c) {
#pragma unroll
        for (int d = 0; d < 4; ++d) {
            o[2 * c][d] = __builtin_amdgcn_perm(r[2 * d + 1][c], r[2 * d][c], 0x05040100u);
            o[2 * c + 1][d] = __builtin_amdgcn_perm(r[2 * d + 1][c], r[2 * d][c], 0x07060302u);
        }
    }
}
constexpr int B_WT = 3 * 64 * 512, B_WOT = 512 * 64, B_WG = 64 * 224, B_WD = 224 * 64;
constexpr int TILE_BLOCKS = B_WT + B_WOT + 2 * B_WG + B_WD;
__device__ __forceinline__ void tile_w_block(int c, const u16* __restrict__ Wq, const u16* __restrict__ Wk, const u16* __restrict__ Wv,
                                             const u16* __restrict__ Wo, const u16* __restrict__ Wg, const u16* __restrict__ Wu,
                                             const u16* __restrict__ Wd, char* __restrict__ ws) {
    const u16* src; int pitch; u16* d0; int dstep;
    bool perm;
    if (c < B_WT) {
        const int nb = c & 511, kb = (c >> 9) & 63, m = c >> 15;
        const int h = nb >> 4, ks = (nb >> 2) & 3, gp = nb & 3, kk = kb >> 2, g = kb & 3;
        src = (m == 0 ? Wq : (m == 1 ? Wk : Wv)) + (size_t)(8 * kb) * 4096 + 8 * nb; pitch = 4096;
        d0 = (u16*)(ws + OFF_WT) + ((size_t)((h * 4 + ks) * 96 + m * 32 + kk) * 64 + g * 16 + 4 * gp) * 8; dstep = 16 * 64 * 8; perm = true;
    } else if ((c -= B_WT) < B_WOT) {
        const int nb = c & 63, kb = c >> 6, h = kb >> 4, ks = (kb >> 2) & 3, g = kb & 3, T = nb >> 1;
        src = Wo + (size_t)(8 * kb) * 512 + 8 * nb; pitch = 512;
        d0 = (u16*)(ws + OFF_WOT) + ((size_t)((h * 32 + T) * 4 + ks) * 64 + g * 16 + 8 * (nb & 1)) * 8; dstep = 0; perm = false;
    } else if ((c -= B_WOT) < 2 * B_WG) {
        const int which = c >= B_WG; if (which) c -= B_WG;
        const int nb = c % 224, kb = c / 224, fp = nb >> 2, gp = nb & 3, kk = kb >> 2, g = kb & 3;
        src = (which ? Wu : Wg) + (size_t)(8 * kb) * FF + 8 * nb; pitch = FF;
        d0 = (u16*)(ws + (which ? OFF_WU : OFF_WG)) + ((size_t)((2 * fp) * 16 + kk) * 64 + g * 16 + 4 * gp) * 8; dstep = 16 * 64 * 8; perm = true;
    } else {
        c -= 2 * B_WG;
        const int nb = c & 63, kb = c >> 6, kk = kb >> 2, g = kb & 3, nt = nb >> 1;
        src = Wd + (size_t)(8 * kb) * 512 + 8 * nb; pitch = 512;
        d0 = (u16*)(ws + OFF_WD) + ((size_t)(nt * 56 + kk) * 64 + g * 16 + 8 * (nb & 1)) * 8; dstep = 0; perm = false;
    }
    v4u r[8], o[8];
#pragma unroll
    for (int j = 0; j < 8; ++j) r[j] = *(const v4u*)(src + (size_t)j * pitch);
    transpose8x8(r, o);
#pragma unroll
    for (int j = 0; j < 8; ++j) *(v4u*)(perm ? d0 + (j >> 2) * dstep + (j & 3) * 8 : d0 + j * 8) = o[j];
}

constexpr int PREP_LN_BLOCKS = S_ / 4, PREP_TILE_BLOCKS = (TILE_BLOCKS + 255) / 256, PREP_ZERO_BLOCKS = 1024;
__global__ __launch_bounds__(256) void k_prep(const u16* __restrict__ x, const u16* __restrict__ Wq, const u16* __restrict__ Wk,
                                              const u16* __restrict__ Wv, const u16* __restrict__ Wo, const u16* __restrict__ Wg,
                                              const u16* __restrict__ Wu, const u16* __restrict__ Wd, char* __restrict__ ws) {
    if (blockIdx.x < PREP_LN_BLOCKS) { ln_row(x, (u16*)(ws + OFF_XN), blockIdx.x * 4 + (threadIdx.x >> 6), threadIdx.x & 63); return; }
    if (blockIdx.x < PREP_LN_BLOCKS + PREP_TILE_BLOCKS) {
        const int c = (blockIdx.x - PREP_LN_BLOCKS) * 256 + threadIdx.x;
        if (c < TILE_BLOCKS) tile_w_block(c, Wq, Wk, Wv, Wo, Wg, Wu, Wd, ws);
        return;
    }
    v4u* z = (v4u*)ws;
    for (size_t e = (size_t)(blockIdx.x - PREP_LN_BLOCKS - PREP_TILE_BLOCKS) * 256 + threadIdx.x; e < ZERO_BYTES / 16; e += (size_t)PREP_ZERO_BLOCKS * 256)
        z[e] = (v4u){0u, 0u, 0u, 0u};
}

#define GLDS16(gp, lp) __builtin_amdgcn_global_load_lds((const unsigned*)(gp), (unsigned*)(lp), 16, 0, 0)
constexpr unsigned QKV_MAT_BYTES = (unsigned)S_ * NH * DH * 2;
__device__ __forceinline__ void qkv_tile(char* lds, const bf16x8* __restrict__ XN, const bf16x8* __restrict__ WT, __amdgpu_buffer_rsrc_t qrs,
                                         int mtile, int m, int h, int w, int lane) {
    bf16x8 (*sm)[32][64] = (bf16x8 (*)[32][64])lds;
    const int wm = w >> 1, wn = w & 1, g = lane >> 4, i = lane & 15;
    const bf16x8* sp = w < 2 ? XN + ((size_t)(mtile * 8 + w * 4) * 16) * 64 + lane
                             : WT + ((size_t)(h * 4 + (w - 2) * 2) * 96 + m * 32) * 64 + lane;
    auto stage = [&](int c, int buf) {
#pragma unroll
        for (int f = 0; f < 8; ++f) {
            const int t = f >> 1, kk = 2 * c + (f & 1);
            const int off = w < 2 ? (t * 16 + kk) * 64 : ((t >> 1) * 96 + (t & 1) * 16 + kk) * 64;
            GLDS16(sp + off, &sm[buf][w * 8 + f][0]);
        }
    };
    f32x4 acc[4][4];
#pragma unroll
    for (int a = 0; a < 4; ++a)
#pragma unroll
        for (int b = 0; b < 4; ++b) acc[a][b] = (f32x4){0.f, 0.f, 0.f, 0.f};
    __syncthreads();
    stage(0, 0);
    for (int c = 0; c < 8; ++c) {
        asm volatile("s_waitcnt vmcnt(0)" ::: "memory");
        __syncthreads();
        if (c + 1 < 8) stage(c + 1, (c + 1) & 1);
#pragma unroll
        for (int k2 = 0; k2 < 2; ++k2) {
            bf16x8 a[4], b[4];
#pragma unroll
            for (int t = 0; t < 4; ++t) { a[t] = sm[c & 1][(wm * 4 + t) * 2 + k2][lane]; b[t] = sm[c & 1][16 + (wn * 4 + t) * 2 + k2][lane]; }
            if (m < 2) {
#pragma unroll
                for (int mt = 0; mt < 4; ++mt)
#pragma unroll
                    for (int t = 0; t < 4; ++t) acc[mt][t] = MFMA(b[t], a[mt], acc[mt][t]);
            } else {
#pragma unroll
                for (int mt = 0; mt < 4; ++mt)
#pragma unroll
                    for (int t = 0; t < 4; ++t) acc[mt][t] = MFMA(a[mt], b[t], acc[mt][t]);
            }
        }
    }
    const int mb = mtile * 2 + wm;
    if (m < 2) {
#pragma unroll
        for (int mt = 0; mt < 4; ++mt) {
            const int tok = mb * 64 + mt * 16 + i, s = tok >> 5, tl = tok & 31;
            const int tile = m == 0 ? (tl >> 4) : ((tl >> 2) & 1), row = m == 0 ? (tl & 15) : (4 * (tl >> 3) + (tl & 3));
#pragma unroll
            for (int p = 0; p < 2; ++p) {
                const int ks = 2 * wn + p;
                v4u o = {pack2(acc[mt][2 * p][0], acc[mt][2 * p][1]), pack2(acc[mt][2 * p][2], acc[mt][2 * p][3]),
                         pack2(acc[mt][2 * p + 1][0], acc[mt][2 * p + 1][1]), pack2(acc[mt][2 * p + 1][2], acc[mt][2 * p + 1][3])};
                __builtin_amdgcn_raw_buffer_store_b128(o, qrs, (unsigned)m * QKV_MAT_BYTES + (unsigned)((s * NH + h) * 8192 + ((tile * 4 + ks) * 64 + g * 16 + row) * 16), 0, 16);
            }
        }
    } else {
#pragma unroll
        for (int mt = 0; mt < 4; ++mt) {
            const int s = mb * 2 + (mt >> 1), gp = 2 * (mt & 1) + (g >> 1);
#pragma unroll
            for (int t = 0; t < 4; ++t) {
                const int tau = 4 * wn + t;
                v2u o = {pack2(acc[mt][t][0], acc[mt][t][1]), pack2(acc[mt][t][2], acc[mt][t][3])};
                __builtin_amdgcn_raw_buffer_store_b64(o, qrs, 2u * QKV_MAT_BYTES + (unsigned)((s * NH + h) * 8192 + (tau * 64 + gp * 16 + i) * 16 + (g & 1) * 8), 0, 16);
            }
        }
    }
}

__device__ __forceinline__ void glu_block(const bf16x8* __restrict__ XN, const bf16x8* __restrict__ WG, const bf16x8* __restrict__ WU,
                                          const bf16x8* __restrict__ WD, u16* __restrict__ Bout, char* lds, int mb, int w, int lane) {
    const int g = lane >> 4, i = lane & 15;
    bf16x8* Hl = (bf16x8*)lds;
    const bf16x8* xa = XN + (size_t)(mb * 2) * 16 * 64 + lane;
    for (int fp = w; fp < 56; fp += 4) {
        f32x4 ag[2][2], au[2][2];
#pragma unroll
        for (int a = 0; a < 2; ++a)
#pragma unroll
            for (int b = 0; b < 2; ++b) { ag[a][b] = (f32x4){0.f, 0.f, 0.f, 0.f}; au[a][b] = (f32x4){0.f, 0.f, 0.f, 0.f}; }
        const bf16x8* wg = WG + (size_t)(fp * 2) * 16 * 64 + lane;
        const bf16x8* wu = WU + (size_t)(fp * 2) * 16 * 64 + lane;
#pragma unroll 4
        for (int kk = 0; kk < 16; ++kk) {
            bf16x8 a[2], bg[2], bu[2];
#pragma unroll
            for (int t = 0; t < 2; ++t) { a[t] = xa[(t * 16 + kk) * 64]; bg[t] = wg[(t * 16 + kk) * 64]; bu[t] = wu[(t * 16 + kk) * 64]; }
#pragma unroll
            for (int mt = 0; mt < 2; ++mt)
#pragma unroll
                for (int t = 0; t < 2; ++t) { ag[mt][t] = MFMA(bg[t], a[mt], ag[mt][t]); au[mt][t] = MFMA(bu[t], a[mt], au[mt][t]); }
        }
#pragma unroll
        for (int mt = 0; mt < 2; ++mt) {
            float hv[8];
#pragma unroll
            for (int t = 0; t < 2; ++t)
#pragma unroll
                for (int r = 0; r < 4; ++r) {
                    const float gb = rbf(ag[mt][t][r]), ub = rbf(au[mt][t][r]);
                    const float sg = rbf(1.0f / (1.0f + __expf(-gb)));
                    hv[t * 4 + r] = sg * ub;
                }
            Hl[(mt * 56 + fp) * 64 + lane] = mk8(pack2(hv[0], hv[1]), pack2(hv[2], hv[3]), pack2(hv[4], hv[5]), pack2(hv[6], hv[7]));
        }
    }
    __syncthreads();
    for (int nq = w; nq < 8; nq += 4) {
        f32x4 ab[2][4];
#pragma unroll
        for (int a = 0; a < 2; ++a)
#pragma unroll
            for (int b = 0; b < 4; ++b) ab[a][b] = (f32x4){0.f, 0.f, 0.f, 0.f};
        const bf16x8* pb = WD + (size_t)(nq * 4) * 56 * 64 + lane;
#pragma unroll 4
        for (int kk = 0; kk < 56; ++kk) {
            const bf16x8 a0 = Hl[kk * 64 + lane], a1 = Hl[(56 + kk) * 64 + lane];
#pragma unroll
            for (int t = 0; t < 4; ++t) {
                const bf16x8 b = pb[(t * 56 + kk) * 64];
                ab[0][t] = MFMA(b, a0, ab[0][t]); ab[1][t] = MFMA(b, a1, ab[1][t]);
            }
        }
#pragma unroll
        for (int mt = 0; mt < 2; ++mt)
#pragma unroll
            for (int t = 0; t < 4; ++t)
                *(v2u*)(Bout + (size_t)(mb * 32 + mt * 16 + i) * DM + (nq * 4 + t) * 16 + 4 * g) =
                    (v2u){pack2(ab[mt][t][0], ab[mt][t][1]), pack2(ab[mt][t][2], ab[mt][t][3])};
    }
}

constexpr int L_WO = 0, L_MEM = 131072, L_SC = L_MEM + 8192, L_OE = L_SC + 6144, L_PX = L_OE + 2048, L_TOTAL = L_PX + 4 * 640;
constexpr unsigned SPINCAP = 1u << 20;
#define JS 4
constexpr int NCH = NH * JS;
constexpr int NWORK = 256 - NCH;
constexpr int TPW = 8 / JS;
constexpr float SCALE = 0.08838834764831845f;
#define SLP1 10
#define AUXS 16
#define AUXL 16
#define SLPN 16
#define SLP2 15

__device__ __forceinline__ float xor32_max(float x) { auto r = __builtin_amdgcn_permlane32_swap(__float_as_uint(x), __float_as_uint(x), false, false); return fmaxf(__uint_as_float(r[0]), __uint_as_float(r[1])); }
__device__ __forceinline__ float xor16_max(float x) { auto r = __builtin_amdgcn_permlane16_swap(__float_as_uint(x), __float_as_uint(x), false, false); return fmaxf(__uint_as_float(r[0]), __uint_as_float(r[1])); }
__device__ __forceinline__ float xor32_add(float x) { auto r = __builtin_amdgcn_permlane32_swap(__float_as_uint(x), __float_as_uint(x), false, false); return __uint_as_float(r[0]) + __uint_as_float(r[1]); }
__device__ __forceinline__ float xor16_add(float x) { auto r = __builtin_amdgcn_permlane16_swap(__float_as_uint(x), __float_as_uint(x), false, false); return __uint_as_float(r[0]) + __uint_as_float(r[1]); }
template <int CTRL> __device__ __forceinline__ float dpp_f(float x) { return __int_as_float(__builtin_amdgcn_update_dpp(0, __float_as_int(x), CTRL, 0xf, 0xf, false)); }
__device__ __forceinline__ float ror8(float x) { return __int_as_float(__builtin_amdgcn_update_dpp(0, __float_as_int(x), 0x128, 0xf, 0xf, false)); }

typedef __attribute__((ext_vector_type(2))) float f32x2;
__device__ __forceinline__ unsigned cvtpk(float a, float b) { unsigned r; asm("v_cvt_pk_bf16_f32 %0, %1, %2" : "=v"(r) : "v"(a), "v"(b)); return r; }
__device__ __forceinline__ float vmax(float a, float b) { float r; asm("v_max_f32 %0, %1, %2" : "=v"(r) : "v"(a), "v"(b)); return r; }
__device__ __forceinline__ float vmax3(float a, float b, float c) { float r; asm("v_max3_f32 %0, %1, %2, %3" : "=v"(r) : "v"(a), "v"(b), "v"(c)); return r; }
__device__ __forceinline__ float xmax32(float x) { auto r = __builtin_amdgcn_permlane32_swap(__float_as_uint(x), __float_as_uint(x), false, false); return vmax(__uint_as_float(r[0]), __uint_as_float(r[1])); }
__device__ __forceinline__ float xmax16(float x) { auto r = __builtin_amdgcn_permlane16_swap(__float_as_uint(x), __float_as_uint(x), false, false); return vmax(__uint_as_float(r[0]), __uint_as_float(r[1])); }
constexpr unsigned KM_BYTES = (unsigned)NSEG * NH * MT * DH * 2;
__device__ __forceinline__ void xattn_unit(__amdgpu_buffer_rsrc_t qrs, __amdgpu_buffer_rsrc_t krs, bf16x8* __restrict__ Oft, int s, int h, int lane) {
    const int g = lane >> 4, i = lane & 15;
    const unsigned qo = (unsigned)((s * NH + h) * 8192 + lane * 16), mo = (unsigned)((s * NH + h) * 2048);
#define LDQ(off) __builtin_bit_cast(bf16x8, __builtin_amdgcn_raw_buffer_load_b128(qrs, (off), 0, 0))
    bf16x8 kf[4], k1[4], k2[4], qf[2][4], vx[8]; v2u vm[8];
#pragma unroll
    for (int ks = 0; ks < 4; ++ks) {
        kf[ks] = __builtin_bit_cast(bf16x8, __builtin_amdgcn_raw_buffer_load_b128(krs, mo + (unsigned)((i & 7) * 256 + 64 * ks + 16 * g), 0, 0));
        k1[ks] = LDQ(QKV_MAT_BYTES + qo + ks * 1024);
        k2[ks] = LDQ(QKV_MAT_BYTES + qo + (4 + ks) * 1024);
        qf[0][ks] = LDQ(qo + ks * 1024);
        qf[1][ks] = LDQ(qo + (4 + ks) * 1024);
    }
#pragma unroll
    for (int tau = 0; tau < 8; ++tau) {
        vx[tau] = LDQ(2u * QKV_MAT_BYTES + qo + tau * 1024);
        vm[tau] = __builtin_amdgcn_raw_buffer_load_b64(krs, KM_BYTES + mo + (unsigned)((tau * 32 + (lane & 31)) * 8), 0, 0);
    }
#pragma unroll
    for (int qt = 0; qt < 2; ++qt) {
        f32x4 sc[3];
#pragma unroll
        for (int a = 0; a < 3; ++a) sc[a] = (f32x4){0.f, 0.f, 0.f, 0.f};
#pragma unroll
        for (int ks = 0; ks < 4; ++ks) {
            sc[0] = MFMA(kf[ks], qf[qt][ks], sc[0]);
            sc[1] = MFMA(k1[ks], qf[qt][ks], sc[1]);
            sc[2] = MFMA(k2[ks], qf[qt][ks], sc[2]);
        }
        const int tq = qt * 16 + i;
        float sv[12]; bool ok[12];
#pragma unroll
        for (int r = 0; r < 4; ++r) { ok[r] = g < 2; ok[4 + r] = (8 * g + r) <= tq; ok[8 + r] = (8 * g + 4 + r) <= tq; }
        float mx = -3.0e38f;
#pragma unroll
        for (int a = 0; a < 3; ++a)
#pragma unroll
            for (int r = 0; r < 4; ++r) { sv[a * 4 + r] = rbf(rbf(sc[a][r]) * SCALE); mx = ok[a * 4 + r] ? fmaxf(mx, sv[a * 4 + r]) : mx; }
        mx = xor16_max(mx);
        mx = xor32_max(mx);
        float ex[12], sum = 0.f;
#pragma unroll
        for (int a = 0; a < 12; ++a) { ex[a] = ok[a] ? __expf(sv[a] - mx) : 0.f; sum += ex[a]; }
        sum = xor16_add(sum);
        sum = xor32_add(sum);
        const float inv = 1.0f / sum;
        const bf16x8 pX = mk8(pack2(ex[4] * inv, ex[5] * inv), pack2(ex[6] * inv, ex[7] * inv), pack2(ex[8] * inv, ex[9] * inv), pack2(ex[10] * inv, ex[11] * inv));
        const bf16x8 pM = mk8(pack2(ex[0] * inv, ex[1] * inv), pack2(ex[2] * inv, ex[3] * inv), 0u, 0u);
#pragma unroll
        for (int ks = 0; ks < 4; ++ks) {
            f32x4 o[2];
#pragma unroll
            for (int t = 0; t < 2; ++t) {
                const int tau = 2 * ks + t;
                o[t] = MFMA(vx[tau], pX, ((f32x4){0.f, 0.f, 0.f, 0.f}));
                o[t] = MFMA(mk8(vm[tau].x, vm[tau].y, 0u, 0u), pM, o[t]);
            }
            Oft[((size_t)(s * 2 + qt) * 128 + h * 4 + ks) * 64 + lane] =
                mk8(pack2(o[0][0], o[0][1]), pack2(o[0][2], o[0][3]), pack2(o[1][0], o[1][1]), pack2(o[1][2], o[1][3]));
        }
    }
}

__global__ __launch_bounds__(256) void k_xattn(u16* QX, u16* KM, bf16x8* __restrict__ Oft) {
    const __amdgpu_buffer_rsrc_t qrs = __builtin_amdgcn_make_buffer_rsrc(QX, 0, 3u * QKV_MAT_BYTES, 0x00020000);
    const __amdgpu_buffer_rsrc_t krs = __builtin_amdgcn_make_buffer_rsrc(KM, 0, 2u * KM_BYTES, 0x00020000);
    const int wid = blockIdx.x * 4 + __builtin_amdgcn_readfirstlane(threadIdx.x >> 6);
    xattn_unit(qrs, krs, Oft, wid >> 5, wid & 31, threadIdx.x & 63);
}

#define AMFMA0(d, a, b) asm volatile("s_nop 1\n\tv_mfma_f32_16x16x32_bf16 %0, %1, %2, 0" : "=&v"(d) : "v"(a), "v"(b))
#define AMFMA(d, a, b) asm volatile("s_nop 1\n\tv_mfma_f32_16x16x32_bf16 %0, %1, %2, %0" : "+v"(d) : "v"(a), "v"(b))
#define AMFMA16(d, a, b) asm volatile("s_nop 1\n\tv_mfma_f32_16x16x16_bf16 %0, %1, %2, %0" : "+v"(d) : "v"(a), "v"(b))

__global__ __launch_bounds__(256, 1) void k_chain(const u16* __restrict__ mem0, const bf16x8* __restrict__ WT, const bf16x8* __restrict__ WOT,
                                                  u16* __restrict__ KM, u16* __restrict__ VM, unsigned* Pbuf, unsigned* Mbuf, unsigned* tmo,
                                                  const bf16x8* __restrict__ XN, const bf16x8* __restrict__ WG, const bf16x8* __restrict__ WU,
                                                  const bf16x8* __restrict__ WD, u16* __restrict__ QX, u16* __restrict__ Bout) {
    extern __shared__ __attribute__((aligned(16))) char lds[];
    const int h = blockIdx.x & 31, jj = blockIdx.x >> 5;
    const int w = __builtin_amdgcn_readfirstlane(threadIdx.x >> 6), lane = threadIdx.x & 63, g = lane >> 4, i = lane & 15;
    const __amdgpu_buffer_rsrc_t qrs = __builtin_amdgcn_make_buffer_rsrc(QX, 0, 3u * QKV_MAT_BYTES, 0x00020000);
    const __amdgpu_buffer_rsrc_t krs = __builtin_amdgcn_make_buffer_rsrc(KM, 0, 2u * KM_BYTES, 0x00020000);
    unsigned* kvcnt = tmo + 16;
    if (blockIdx.x >= NCH) {
        const int wb = blockIdx.x - NCH;
        for (int t = wb; t < 1024; t += NWORK) {
            qkv_tile(lds, XN, WT, qrs, t >> 6, 1 + ((t >> 5) & 1), t & 31, w, lane);
            asm volatile("s_waitcnt vmcnt(0)" ::: "memory");
            __syncthreads();
            if (threadIdx.x == 0) __hip_atomic_fetch_add(kvcnt + (t >> 6), 1u, __ATOMIC_RELAXED, __HIP_MEMORY_SCOPE_AGENT);
        }
        __syncthreads();
        if (wb < 64) {
            glu_block(XN, WG, WU, WD, Bout, lds, wb, w, lane);
        } else {
            for (int t = wb - 64; t < 512; t += NWORK - 64) {
                qkv_tile(lds, XN, WT, qrs, t >> 5, 0, t & 31, w, lane);
            }
        }
        return;
    }
    unsigned kv_ready = 0;
    auto wait_kv = [&](int grp) {
        unsigned spins = 0;
        while (!((kv_ready >> grp) & 1u)) {
            const unsigned c = __hip_atomic_load(kvcnt + (lane & 15), __ATOMIC_RELAXED, __HIP_MEMORY_SCOPE_AGENT);
            kv_ready = (unsigned)__ballot(c >= 64u) & 0xffffu;
            if ((kv_ready >> grp) & 1u) break;
            __builtin_amdgcn_s_sleep(2);
            if (++spins > SPINCAP) { if (lane == 0) atomicAdd(tmo, 1u); break; }
        }
    };
    auto fetch_kv = [&](int sg, v4u (&pk)[2], v4u (&pv)[2]) {
        const unsigned o = (unsigned)((sg * NH + h) * 8192 + lane * 16);
        pk[0] = __builtin_amdgcn_raw_buffer_load_b128(qrs, QKV_MAT_BYTES + o + w * 1024, 0, 16);
        pk[1] = __builtin_amdgcn_raw_buffer_load_b128(qrs, QKV_MAT_BYTES + o + w * 1024 + 4096, 0, 16);
        pv[0] = __builtin_amdgcn_raw_buffer_load_b128(qrs, 2u * QKV_MAT_BYTES + o + w * 2048, 0, 16);
        pv[1] = __builtin_amdgcn_raw_buffer_load_b128(qrs, 2u * QKV_MAT_BYTES + o + w * 2048 + 1024, 0, 16);
    };
    const __amdgpu_buffer_rsrc_t prs = __builtin_amdgcn_make_buffer_rsrc(Pbuf, 0, 63 * 32 * 16384, 0x00020000);
    const __amdgpu_buffer_rsrc_t mrs = __builtin_amdgcn_make_buffer_rsrc(Mbuf, 0, 64 * 8192, 0x00020000);

    bf16x8 WA[64], WV[32];
    {
        const bf16x8* src = WT + (size_t)(h * 4 + w) * 96 * 64 + lane;
#pragma unroll
        for (int f = 0; f < 64; ++f) WA[f] = src[f * 64];
#pragma unroll
        for (int f = 0; f < 32; ++f) WV[f] = src[(64 + f) * 64];
        const bf16x8* so = WOT + ((size_t)h * 128 + (jj * 4 + w) * TPW * 4) * 64 + lane;
        bf16x8* lo = (bf16x8*)(lds + L_WO) + (w * TPW * 4) * 64 + lane;
#pragma unroll 8
        for (int f = 0; f < TPW * 4; ++f) lo[f * 64] = so[f * 64];
    }
    {
        const int tid = threadIdx.x;
#pragma unroll
        for (int c = tid; c < 512; c += 256) {
            const int t = c & 7, gg = (c >> 3) & 3, kk = c >> 5;
            *(v4u*)(lds + L_MEM + c * 16) = *(const v4u*)(mem0 + t * DM + 32 * kk + 8 * gg);
        }
    }
    v4u pk[2], pv[2];
    wait_kv(0);
    fetch_kv(0, pk, pv);
    const int rid = blockIdx.x * 4 + w;
    const bool is_red = (rid % JS) == 0;
    const int red = rid / JS;
#define STAMP(k)
    for (int s = 0; s < NSEG; ++s) {
        __syncthreads();
        STAMP(0);
        asm volatile("" : "+v"(pk[0]), "+v"(pk[1]), "+v"(pv[0]), "+v"(pv[1]));
        f32x4 acc[6];
        bf16x8 mf[16];
        const char* mp = lds + L_MEM + g * 128 + (i & 7) * 16;
        {
#pragma unroll
            for (int kk = 0; kk < 4; ++kk) mf[kk] = *(const bf16x8*)(mp + kk * 512);
#pragma unroll
            for (int kk = 0; kk < 16; ++kk) {
                if (kk + 4 < 16) mf[kk + 4] = *(const bf16x8*)(mp + (kk + 4) * 512);
                if (kk == 0) {
                    asm volatile("v_mfma_f32_16x16x32_bf16 %0, %4, %8, 0\n\tv_mfma_f32_16x16x32_bf16 %1, %5, %8, 0\n\t"
                                 "v_mfma_f32_16x16x32_bf16 %2, %6, %8, 0\n\tv_mfma_f32_16x16x32_bf16 %3, %7, %8, 0"
                                 : "=&v"(acc[0]), "=&v"(acc[1]), "=&v"(acc[2]), "=&v"(acc[3])
                                 : "a"(WA[0]), "a"(WA[16]), "a"(WA[32]), "a"(WA[48]), "v"(mf[0]));
                } else {
                    asm volatile("v_mfma_f32_16x16x32_bf16 %0, %4, %8, %0\n\tv_mfma_f32_16x16x32_bf16 %1, %5, %8, %1\n\t"
                                 "v_mfma_f32_16x16x32_bf16 %2, %6, %8, %2\n\tv_mfma_f32_16x16x32_bf16 %3, %7, %8, %3"
                                 : "+v"(acc[0]), "+v"(acc[1]), "+v"(acc[2]), "+v"(acc[3])
                                 : "a"(WA[kk]), "a"(WA[16 + kk]), "a"(WA[32 + kk]), "a"(WA[48 + kk]), "v"(mf[kk]));
                }
            }
            asm volatile("s_nop 7\n\ts_nop 1" : "+v"(acc[0]), "+v"(acc[1]), "+v"(acc[2]), "+v"(acc[3]));
        }
        const bf16x8 qf = mk8(pack2(acc[0][0], acc[0][1]), pack2(acc[0][2], acc[0][3]), pack2(acc[1][0], acc[1][1]), pack2(acc[1][2], acc[1][3]));
        const bf16x8 kf = mk8(pack2(acc[2][0], acc[2][1]), pack2(acc[2][2], acc[2][3]), pack2(acc[3][0], acc[3][1]), pack2(acc[3][2], acc[3][3]));
        if (i < 8 && jj == 0) __builtin_amdgcn_raw_buffer_store_b128(__builtin_bit_cast(v4u, kf), krs, (unsigned)((s * NH + h) * 2048 + i * 256 + 64 * w + 16 * g), 0, 0);
        {
            f32x4 sp[3];
            AMFMA0(sp[0], kf, qf); AMFMA0(sp[1], pk[0], qf); AMFMA0(sp[2], pk[1], qf);
            asm volatile("s_nop 7" : "+v"(sp[0]), "+v"(sp[1]), "+v"(sp[2]));
            if (i < 8) {
#pragma unroll
                for (int a = 0; a < 3; ++a) *(f32x4*)(lds + L_SC + (((w * 3 + a) * 4 + g) * 8 + i) * 16) = sp[a];
            }
        }
#pragma unroll
        for (int kk = 0; kk < 4; ++kk) mf[kk] = *(const bf16x8*)(mp + kk * 512);
        STAMP(1);
        __syncthreads();
        STAMP(2);
#define VS(kk, hf)                                                                                                             \
        do {                                                                                                                   \
            if ((hf) == 0 && (kk) + 4 < 16) mf[((kk) + 4) & 15] = *(const bf16x8*)(mp + ((kk) + 4) * 512);                     \
            if ((kk) == 0) asm volatile("v_mfma_f32_16x16x32_bf16 %0, %2, %1, 0" : "=&v"(acc[4 + (hf)]) : "v"(WV[16 * (hf)]), "v"(mf[0]));      \
            else asm volatile("v_mfma_f32_16x16x32_bf16 %0, %2, %1, %0" : "+v"(acc[4 + (hf)]) : "v"(WV[16 * (hf) + (kk)]), "v"(mf[kk]));        \
        } while (0)
        v2u v0, v1;
        bf16x8 wf[4][8];
        const char* wp = lds + L_WO + (w * TPW * 4) * 1024 + lane * 16;
        {
            const int q = lane & 7, kg = lane >> 3;
            const char* sx = lds + L_SC + (((1 + (kg & 1)) * 4 + (kg >> 1)) * 8 + q) * 16;
            const char* sm_ = lds + L_SC + ((kg >> 2) * 8 + q) * 16 + (kg & 3) * 4;
            f32x4 px[4]; float pm[4];
#pragma unroll
            for (int ww = 0; ww < 4; ++ww) { px[ww] = *(const f32x4*)(sx + ww * 1536); pm[ww] = *(const float*)(sm_ + ww * 1536); }
            VS(0, 0); VS(0, 1); VS(1, 0); VS(1, 1); VS(2, 0); VS(2, 1); VS(3, 0); VS(3, 1);
            const f32x4 tx = (px[0] + px[1]) + (px[2] + px[3]);
            VS(4, 0);
            const float tm = (pm[0] + pm[1]) + (pm[2] + pm[3]);
            VS(4, 1);
            const unsigned u0 = cvtpk(tx[0], tx[1]), u1 = cvtpk(tx[2], tx[3]), u2 = cvtpk(tm, tm);
            VS(5, 0);
            const f32x4 bx = (f32x4){__uint_as_float(u0 << 16), __uint_as_float(u0 & 0xffff0000u), __uint_as_float(u1 << 16), __uint_as_float(u1 & 0xffff0000u)} * SCALE;
            const float bm = __uint_as_float(u2 << 16) * SCALE;
            VS(5, 1);
            const unsigned w0 = cvtpk(bx[0], bx[1]), w1 = cvtpk(bx[2], bx[3]), w2 = cvtpk(bm, bm);
            VS(6, 0);
            const float s0 = __uint_as_float(w0 << 16), s1 = __uint_as_float(w0 & 0xffff0000u), s2 = __uint_as_float(w1 << 16),
                        s3 = __uint_as_float(w1 & 0xffff0000u), s4 = __uint_as_float(w2 << 16);
            VS(6, 1);
            float mx = vmax3(vmax3(s0, s1, s2), s3, s4);
            VS(7, 0);
            mx = vmax(mx, ror8(mx)); VS(7, 1);
            mx = xmax16(mx); VS(8, 0);
            mx = xmax32(mx); VS(8, 1);
            constexpr float L2E = 1.4426950408889634f;
            const float nm = -mx * L2E;
            const float e0 = __builtin_amdgcn_exp2f(__builtin_fmaf(s0, L2E, nm)), e1 = __builtin_amdgcn_exp2f(__builtin_fmaf(s1, L2E, nm));
            VS(9, 0);
            const float e2 = __builtin_amdgcn_exp2f(__builtin_fmaf(s2, L2E, nm)), e3 = __builtin_amdgcn_exp2f(__builtin_fmaf(s3, L2E, nm));
            VS(9, 1);
            const float e4 = __builtin_amdgcn_exp2f(__builtin_fmaf(s4, L2E, nm));
            float sum = ((e0 + e1) + (e2 + e3)) + e4;
            VS(10, 0);
            sum += ror8(sum); VS(10, 1);
            sum = xor16_add(sum); VS(11, 0);
            sum = xor32_add(sum); VS(11, 1);
            const float inv = __builtin_amdgcn_rcpf(sum);
            char* pw = lds + L_PX + w * 640;
            *(v2u*)(pw + q * 64 + kg * 8) = (v2u){cvtpk(e0 * inv, e1 * inv), cvtpk(e2 * inv, e3 * inv)};
            *(u16*)(pw + 512 + q * 16 + kg * 2) = (u16)cvtpk(e4 * inv, e4 * inv);
            VS(12, 0); VS(12, 1); VS(13, 0); VS(13, 1);
            __builtin_amdgcn_wave_barrier();
            const bf16x8 pX = *(const bf16x8*)(pw + (i & 7) * 64 + g * 16);
            v2u pM = *(const v2u*)(pw + 512 + (i & 7) * 16 + (g & 1) * 8);
#pragma unroll
            for (int f = 0; f < 8; ++f) wf[0][f] = *(const bf16x8*)(wp + f * 1024);
            VS(14, 0); VS(14, 1); VS(15, 0); VS(15, 1);
            asm volatile("s_nop 7" : "+v"(acc[4]), "+v"(acc[5]));
            v0 = (v2u){cvtpk(acc[4][0], acc[4][1]), cvtpk(acc[4][2], acc[4][3])};
            v1 = (v2u){cvtpk(acc[5][0], acc[5][1]), cvtpk(acc[5][2], acc[5][3])};
            if (g >= 2) pM = (v2u){0u, 0u};
            f32x4 o0, o1;
            AMFMA0(o0, pv[0], pX); AMFMA0(o1, pv[1], pX);
            asm volatile("s_nop 5" : "+v"(o0), "+v"(o1));
            AMFMA16(o0, v0, pM); AMFMA16(o1, v1, pM);
            asm volatile("s_nop 7" : "+v"(o0), "+v"(o1));
            if (i < 8) *(bf16x8*)(lds + L_OE + ((w * 4 + g) * 8 + i) * 16) = mk8(cvtpk(o0[0], o0[1]), cvtpk(o0[2], o0[3]), cvtpk(o1[0], o1[1]), cvtpk(o1[2], o1[3]));
        }
        if (g < 2 && jj == 0) {
            __builtin_amdgcn_raw_buffer_store_b64(v0, krs, KM_BYTES + (unsigned)((s * NH + h) * 2048 + (2 * w * 32 + lane) * 8), 0, 0);
            __builtin_amdgcn_raw_buffer_store_b64(v1, krs, KM_BYTES + (unsigned)((s * NH + h) * 2048 + ((2 * w + 1) * 32 + lane) * 8), 0, 0);
        }
        if (s == NSEG - 1) break;
        const unsigned pofs = (unsigned)s * (32u * 16384u);
        {
            __syncthreads();
            STAMP(3);
            bf16x8 of[4];
#pragma unroll
            for (int ks = 0; ks < 4; ++ks) of[ks] = *(const bf16x8*)(lds + L_OE + ((ks * 4 + g) * 8 + (i & 7)) * 16);
#pragma unroll
            for (int a = 0; a < TPW / 2; ++a) {
                if (a + 1 < TPW / 2) {
#pragma unroll
                    for (int f = 0; f < 8; ++f) wf[a + 1][f] = *(const bf16x8*)(wp + ((a + 1) * 8 + f) * 1024);
                }
                f32x4 d0, d1;
                AMFMA0(d0, wf[a][0], of[0]); AMFMA0(d1, wf[a][4], of[0]);
#pragma unroll
                for (int ks = 1; ks < 4; ++ks) { AMFMA(d0, wf[a][ks], of[ks]); AMFMA(d1, wf[a][4 + ks], of[ks]); }
                asm volatile("s_nop 7" : "+v"(d0), "+v"(d1));
                v4u v;
#pragma unroll
                for (int r = 0; r < 4; ++r) {
                    const float x1 = ror8(d1[r]);
                    v[r] = __float_as_uint(i < 8 ? d0[r] : x1) | 1u;
                }
                const int T = (jj * 4 + w) * TPW + 2 * a + (i >> 3);
                __builtin_amdgcn_raw_buffer_store_b128(v, prs, pofs + (unsigned)(h * 16384 + (4 * T + g) * 128 + (i & 7) * 16), 0, AUXS);
            }
        }
        STAMP(4);
        if (((s + 1) & 3) == 0) wait_kv((s + 1) >> 2);
        fetch_kv(s + 1, pk, pv);
        for (int z = 0; z < SLP1; ++z) __builtin_amdgcn_s_sleep(1);
        if (!is_red) {
            for (int z = 0; z < SLPN; ++z) __builtin_amdgcn_s_sleep(1);
        } else {
            v4u r[4];
            unsigned spins = 0;
            for (;;) {
#pragma unroll
                for (int q = 0; q < 4; ++q)
                    r[q] = __builtin_amdgcn_raw_buffer_load_b128(prs, pofs + (unsigned)((q * 8 + (lane >> 3)) * 16384 + red * 128 + (lane & 7) * 16), 0, AUXL);
                bool ok = true;
#pragma unroll
                for (int q = 0; q < 4; ++q) ok &= (r[q].x & r[q].y & r[q].z & r[q].w & 1u) != 0u;
                if (__all(ok)) break;
                if (++spins > SPINCAP) { if (lane == 0) atomicAdd(tmo, 1u); break; }
            }
            STAMP(5);
            float sm[4];
#pragma unroll
            for (int c = 0; c < 4; ++c) {
                sm[c] = (__uint_as_float(r[0][c] & ~1u) + __uint_as_float(r[1][c] & ~1u)) + (__uint_as_float(r[2][c] & ~1u) + __uint_as_float(r[3][c] & ~1u));
                sm[c] += ror8(sm[c]);
                sm[c] = xor16_add(sm[c]);
                sm[c] = xor32_add(sm[c]);
            }
            if (lane < 8) {
                v2u v = {pack2(sm[0], sm[1]), pack2(sm[2], sm[3])};
                if (v.x == 0u) v.x = 0x8000u;
                if (v.y == 0u) v.y = 0x8000u;
                __builtin_amdgcn_raw_buffer_store_b64(v, mrs, (unsigned)(s * 8192 + red * 64 + lane * 8), 0, AUXS);
            }
        }
        STAMP(6);
        for (int z = 0; z < SLP2; ++z) __builtin_amdgcn_s_sleep(1);
        {
            v4u m[2];
            unsigned spins = 0;
            for (;;) {
#pragma unroll
                for (int q = 0; q < 2; ++q) m[q] = __builtin_amdgcn_raw_buffer_load_b128(mrs, (unsigned)(s * 8192 + w * 2048 + q * 1024 + lane * 16), 0, AUXL);
                bool ok = true;
#pragma unroll
                for (int q = 0; q < 2; ++q) ok &= (m[q].x != 0u) & (m[q].y != 0u) & (m[q].z != 0u) & (m[q].w != 0u);
                if (__all(ok)) break;
                if (++spins > SPINCAP) { if (lane == 0) atomicAdd(tmo, 1u); break; }
            }
#pragma unroll
            for (int q = 0; q < 2; ++q) {
                const int R = w * 32 + q * 16 + (lane >> 2), t = 2 * (lane & 3);
                char* dst = lds + L_MEM + (R >> 3) * 512 + ((R >> 1) & 3) * 128 + t * 16 + (R & 1) * 8;
                *(v2u*)dst = (v2u){m[q].x, m[q].y};
                *(v2u*)(dst + 16) = (v2u){m[q].z, m[q].w};
            }
        }
        STAMP(7);
    }
}

__global__ __launch_bounds__(256, 1) void k_final(const bf16x8* __restrict__ Oft, const bf16x8* __restrict__ WOT, f32x4* __restrict__ part) {
    __shared__ bf16x8 sm[4][32][64];
    const int w = __builtin_amdgcn_readfirstlane(threadIdx.x >> 6), lane = threadIdx.x & 63;
    const int xcd = blockIdx.x & 7, j = blockIdx.x >> 3, mtile = xcd * 2 + (j >> 4), ntile = (j >> 2) & 3, kq = j & 3;
    const int wm = w >> 1, wn = w & 1;
    const bf16x8* sp = w < 2 ? Oft + ((size_t)(mtile * 8 + w * 4) * 128 + kq * 32) * 64 + lane
                             : WOT + (((size_t)(kq * 8) * 32 + ntile * 8 + (w - 2) * 4) * 4) * 64 + lane;
    auto stage = [&](int c, int buf) {
#pragma unroll
        for (int f = 0; f < 8; ++f) {
            const int t = f >> 1, kk = 2 * c + (f & 1);
            const int off = w < 2 ? (t * 128 + kk) * 64 : (((kk >> 2) * 32 + t) * 4 + (kk & 3)) * 64;
            GLDS16(sp + off, &sm[buf][w * 8 + f][0]);
        }
    };
    f32x4 acc[4][4];
#pragma unroll
    for (int a = 0; a < 4; ++a)
#pragma unroll
        for (int b = 0; b < 4; ++b) acc[a][b] = (f32x4){0.f, 0.f, 0.f, 0.f};
    stage(0, 0); stage(1, 1); stage(2, 2);
#pragma unroll
    for (int c = 0; c < 16; ++c) {
        if (c < 14) asm volatile("s_waitcnt vmcnt(16)\n\ts_barrier" ::: "memory");
        else if (c == 14) asm volatile("s_waitcnt vmcnt(8)\n\ts_barrier" ::: "memory");
        else asm volatile("s_waitcnt vmcnt(0)\n\ts_barrier" ::: "memory");
        if (c + 3 < 16) stage(c + 3, (c + 3) & 3);
#pragma unroll
        for (int k2 = 0; k2 < 2; ++k2) {
            bf16x8 a[4], b[4];
#pragma unroll
            for (int t = 0; t < 4; ++t) { a[t] = sm[c & 3][(wm * 4 + t) * 2 + k2][lane]; b[t] = sm[c & 3][16 + (wn * 4 + t) * 2 + k2][lane]; }
#pragma unroll
            for (int mt = 0; mt < 4; ++mt)
#pragma unroll
                for (int t = 0; t < 4; ++t) acc[mt][t] = MFMA(b[t], a[mt], acc[mt][t]);
        }
    }
#pragma unroll
    for (int mt = 0; mt < 4; ++mt)
#pragma unroll
        for (int t = 0; t < 4; ++t)
            part[(((size_t)kq * 128 + mtile * 8 + wm * 4 + mt) * 32 + ntile * 8 + wn * 4 + t) * 64 + lane] = acc[mt][t];
}

__global__ __launch_bounds__(256) void k_out(const f32x4* __restrict__ part, const u16* __restrict__ Bout, const u16* __restrict__ x,
                                             u16* __restrict__ out) {
    const int id = blockIdx.x * 256 + threadIdx.x, lane = id & 63, tile = id >> 6, g = lane >> 4, i = lane & 15;
    constexpr size_t KS = (size_t)128 * 32 * 64;
    const f32x4 a = (part[id] + part[KS + id]) + (part[2 * KS + id] + part[3 * KS + id]);
    const size_t idx = (size_t)((tile >> 5) * 16 + i) * DM + (tile & 31) * 16 + 4 * g;
    const v2u xr = *(const v2u*)(x + idx), br = *(const v2u*)(Bout + idx);
    const float xv[4] = {__uint_as_float(xr.x << 16), __uint_as_float(xr.x & 0xffff0000u), __uint_as_float(xr.y << 16), __uint_as_float(xr.y & 0xffff0000u)};
    const float bv[4] = {__uint_as_float(br.x << 16), __uint_as_float(br.x & 0xffff0000u), __uint_as_float(br.y << 16), __uint_as_float(br.y & 0xffff0000u)};
    float y[4];
#pragma unroll
    for (int r = 0; r < 4; ++r) y[r] = rbf(xv[r] + rbf(a[r])) + bv[r];
    *(v2u*)(out + idx) = (v2u){pack2(y[0], y[1]), pack2(y[2], y[3])};
}

extern "C" void kernel_launch(void* const* d_in, const int* in_sizes, int n_in, void* d_out, int out_size, void* d_ws, size_t ws_size,
                              hipStream_t stream) {
    const u16* x = (const u16*)d_in[0];
    const u16* mem0 = (const u16*)d_in[1];
    const u16* Wq = (const u16*)d_in[2];
    const u16* Wk = (const u16*)d_in[3];
    const u16* Wv = (const u16*)d_in[4];
    const u16* Wo = (const u16*)d_in[5];
    const u16* Wg = (const u16*)d_in[8];
    const u16* Wu = (const u16*)d_in[9];
    const u16* Wd = (const u16*)d_in[10];
    char* ws = (char*)d_ws;
    (void)hipFuncSetAttribute((const void*)k_chain, hipFuncAttributeMaxDynamicSharedMemorySize, L_TOTAL);
    k_prep<<<PREP_LN_BLOCKS + PREP_TILE_BLOCKS + PREP_ZERO_BLOCKS, 256, 0, stream>>>(x, Wq, Wk, Wv, Wo, Wg, Wu, Wd, ws);
    k_chain<<<NCH + NWORK, 256, L_TOTAL, stream>>>(mem0, (const bf16x8*)(ws + OFF_WT), (const bf16x8*)(ws + OFF_WOT), (u16*)(ws + OFF_KM), (u16*)(ws + OFF_VM), (unsigned*)(ws + OFF_P),
                                                  (unsigned*)(ws + OFF_M), (unsigned*)(ws + OFF_TMO), (const bf16x8*)(ws + OFF_XN),
                                                  (const bf16x8*)(ws + OFF_WG), (const bf16x8*)(ws + OFF_WU), (const bf16x8*)(ws + OFF_WD),
                                                  (u16*)(ws + OFF_QX), (u16*)(ws + OFF_H));
    k_xattn<<<NSEG * NH / 4, 256, 0, stream>>>((u16*)(ws + OFF_QX), (u16*)(ws + OFF_KM), (bf16x8*)(ws + OFF_O));
    k_final<<<256, 256, 0, stream>>>((const bf16x8*)(ws + OFF_O), (const bf16x8*)(ws + OFF_WOT), (f32x4*)(ws + OFF_PART));
    k_out<<<S_ * DM / 4 / 256, 256, 0, stream>>>((const f32x4*)(ws + OFF_PART), (const u16*)(ws + OFF_H), x, (u16*)d_out);
}
```

```cpp
#include <hip/hip_runtime.h>
#include <hip/hip_bf16.h>

constexpr int S_ = 2048, DM = 512, NH = 32, DH = 128, MT = 8, SEG = 32, NSEG = 64, FF = 1792;

typedef unsigned short u16;
typedef __attribute__((ext_vector_type(8))) short bf16x8;
typedef __attribute__((ext_vector_type(4))) float f32x4;
typedef __attribute__((ext_vector_type(4))) unsigned v4u;
typedef __attribute__((ext_vector_type(2))) unsigned v2u;
typedef __bf16 bf16x2_t __attribute__((ext_vector_type(2)));

__device__ __forceinline__ unsigned pack2(float a, float b) {
    bf16x2_t v = {(__bf16)a, (__bf16)b};
    return __builtin_bit_cast(unsigned, v);
}
__device__ __forceinline__ float rbf(float a) { return (float)(__bf16)a; }
__device__ __forceinline__ float bf2f(u16 b) { return __uint_as_float(((unsigned)b) << 16); }
__device__ __forceinline__ bf16x8 mk8(unsigned a, unsigned b, unsigned c, unsigned d) {
    v4u v = {a, b, c, d};
    return __builtin_bit_cast(bf16x8, v);
}
#define MFMA(a, b, c) __builtin_amdgcn_mfma_f32_16x16x32_bf16((a), (b), (c), 0, 0, 0)

__device__ __forceinline__ int pi_col(int tau, int rho) { return 32 * (tau >> 1) + 8 * (rho >> 2) + 4 * (tau & 1) + (rho & 3); }

constexpr size_t OFF_TMO = 0;
constexpr size_t OFF_M   = 65536;
constexpr size_t OFF_P   = OFF_M + 64 * 8192;
constexpr size_t ZERO_BYTES = OFF_P + (size_t)63 * 32 * 16384;
constexpr size_t OFF_XN  = (ZERO_BYTES + 4095) / 4096 * 4096;
constexpr size_t OFF_WT  = OFF_XN + (size_t)S_ * DM * 2;
constexpr size_t OFF_WOT = OFF_WT + (size_t)3 * DM * NH * DH * 2;
constexpr size_t OFF_WG  = OFF_WOT + (size_t)DM * NH * DH * 2;
constexpr size_t OFF_WU  = OFF_WG + (size_t)DM * FF * 2;
constexpr size_t OFF_WD  = OFF_WU + (size_t)DM * FF * 2;
constexpr size_t OFF_QX  = OFF_WD + (size_t)DM * FF * 2;
constexpr size_t OFF_KX  = OFF_QX + (size_t)S_ * NH * DH * 2;
constexpr size_t OFF_VXT = OFF_KX + (size_t)S_ * NH * DH * 2;
constexpr size_t OFF_KM  = OFF_VXT + (size_t)S_ * NH * DH * 2;
constexpr size_t OFF_VM  = OFF_KM + (size_t)NSEG * NH * MT * DH * 2;
constexpr size_t OFF_H   = OFF_VM + (size_t)NSEG * NH * MT * DH * 2;
constexpr size_t OFF_O   = OFF_H + (size_t)S_ * FF * 2;
constexpr size_t OFF_PART = OFF_O + (size_t)S_ * NH * DH * 2;
constexpr size_t WS_TOTAL = OFF_PART + (size_t)8 * S_ * DM * 4;

__device__ __forceinline__ void ln_row(const u16* __restrict__ x, u16* __restrict__ xn_ft, int row, int lane) {
    const v4u raw = *(const v4u*)(x + (size_t)row * DM + lane * 8);
    float v[8];
    v[0] = __uint_as_float(raw.x << 16); v[1] = __uint_as_float(raw.x & 0xffff0000u);
    v[2] = __uint_as_float(raw.y << 16); v[3] = __uint_as_float(raw.y & 0xffff0000u);
    v[4] = __uint_as_float(raw.z << 16); v[5] = __uint_as_float(raw.z & 0xffff0000u);
    v[6] = __uint_as_float(raw.w << 16); v[7] = __uint_as_float(raw.w & 0xffff0000u);
    float s = 0.f;
#pragma unroll
    for (int j = 0; j < 8; ++j) s += v[j];
#pragma unroll
    for (int o = 1; o < 64; o <<= 1) s += __shfl_xor(s, o);
    const float mean = s * (1.0f / DM);
    float q = 0.f;
#pragma unroll
    for (int j = 0; j < 8; ++j) { const float d = v[j] - mean; q += d * d; }
#pragma unroll
    for (int o = 1; o < 64; o <<= 1) q += __shfl_xor(q, o);
    const float mb = rbf(mean), vb = rbf(q * (1.0f / DM));
    const float den = rbf(sqrtf(rbf(vb + 1e-05f)));
    float y[8];
#pragma unroll
    for (int j = 0; j < 8; ++j) y[j] = rbf(v[j] - mb) / den;
    v4u o4 = {pack2(y[0], y[1]), pack2(y[2], y[3]), pack2(y[4], y[5]), pack2(y[6], y[7])};
    const int mt = row >> 4, kk = lane >> 2, fl = (lane & 3) * 16 + (row & 15);
    *(v4u*)(xn_ft + (((size_t)mt * 16 + kk) * 64 + fl) * 8) = o4;
}

__device__ __forceinline__ void transpose8x8(const v4u (&r)[8], v4u (&o)[8]) {
#pragma unroll
    for (int c = 0; c < 4; ++c) {
#pragma unroll
        for (int d = 0; d < 4; ++d) {
            o[2 * c][d] = __builtin_amdgcn_perm(r[2 * d + 1][c], r[2 * d][c], 0x05040100u);
            o[2 * c + 1][d] = __builtin_amdgcn_perm(r[2 * d + 1][c], r[2 * d][c], 0x07060302u);
        }
    }
}
constexpr int B_WT = 3 * 64 * 512, B_WOT = 512 * 64, B_WG = 64 * 224, B_WD = 224 * 64;
constexpr int TILE_A = B_WT + B_WOT, TILE_BLOCKS = TILE_A + 2 * B_WG + B_WD;
__device__ __forceinline__ void tile_w_block(int c, const u16* __restrict__ Wq, const u16* __restrict__ Wk, const u16* __restrict__ Wv,
                                             const u16* __restrict__ Wo, const u16* __restrict__ Wg, const u16* __restrict__ Wu,
                                             const u16* __restrict__ Wd, char* __restrict__ ws, bool wt) {
    const u16* src; int pitch; u16* d0; int dstep;
    bool perm;
    if (c < B_WT) {
        const int nb = c & 511, kb = (c >> 9) & 63, m = c >> 15;
        const int h = nb >> 4, ks = (nb >> 2) & 3, gp = nb & 3, kk = kb >> 2, g = kb & 3;
        src = (m == 0 ? Wq : (m == 1 ? Wk : Wv)) + (size_t)(8 * kb) * 4096 + 8 * nb; pitch = 4096;
        d0 = (u16*)(ws + OFF_WT) + ((size_t)((h * 4 + ks) * 96 + m * 32 + kk) * 64 + g * 16 + 4 * gp) * 8; dstep = 16 * 64 * 8; perm = true;
    } else if ((c -= B_WT) < B_WOT) {
        const int nb = c & 63, kb = c >> 6, h = kb >> 4, ks = (kb >> 2) & 3, g = kb & 3, T = nb >> 1;
        src = Wo + (size_t)(8 * kb) * 512 + 8 * nb; pitch = 512;
        d0 = (u16*)(ws + OFF_WOT) + ((size_t)((h * 32 + T) * 4 + ks) * 64 + g * 16 + 8 * (nb & 1)) * 8; dstep = 0; perm = false;
    } else if ((c -= B_WOT) < 2 * B_WG) {
        const int which = c >= B_WG; if (which) c -= B_WG;
        const int nb = c % 224, kb = c / 224, fp = nb >> 2, gp = nb & 3, kk = kb >> 2, g = kb & 3;
        src = (which ? Wu : Wg) + (size_t)(8 * kb) * FF + 8 * nb; pitch = FF;
        d0 = (u16*)(ws + (which ? OFF_WU : OFF_WG)) + ((size_t)((2 * fp) * 16 + kk) * 64 + g * 16 + 4 * gp) * 8; dstep = 16 * 64 * 8; perm = true;
    } else {
        c -= 2 * B_WG;
        const int nb = c & 63, kb = c >> 6, kk = kb >> 2, g = kb & 3, nt = nb >> 1;
        src = Wd + (size_t)(8 * kb) * 512 + 8 * nb; pitch = 512;
        d0 = (u16*)(ws + OFF_WD) + ((size_t)(nt * 56 + kk) * 64 + g * 16 + 8 * (nb & 1)) * 8; dstep = 0; perm = false;
    }
    v4u r[8], o[8];
#pragma unroll
    for (int j = 0; j < 8; ++j) r[j] = *(const v4u*)(src + (size_t)j * pitch);
    transpose8x8(r, o);
    if (wt) {
        const __amdgpu_buffer_rsrc_t wrs = __builtin_amdgcn_make_buffer_rsrc(ws, 0, (unsigned)WS_TOTAL, 0x00020000);
        const unsigned b0 = (unsigned)((char*)d0 - ws);
#pragma unroll
        for (int j = 0; j < 8; ++j) __builtin_amdgcn_raw_buffer_store_b128(o[j], wrs, b0 + (unsigned)(perm ? (j >> 2) * dstep + (j & 3) * 8 : j * 8) * 2u, 0, 16);
    } else {
#pragma unroll
        for (int j = 0; j < 8; ++j) *(v4u*)(perm ? d0 + (j >> 2) * dstep + (j & 3) * 8 : d0 + j * 8) = o[j];
    }
}

constexpr int PREP_LN_BLOCKS = S_ / 4, PREP_TILE_BLOCKS = (TILE_A + 255) / 256, PREP_ZERO_BLOCKS = 256;
constexpr int ZSTEPS = 12;
constexpr size_t ZERO_A = OFF_P + (size_t)ZSTEPS * 32 * 16384;
__global__ __launch_bounds__(256) void k_prep(const u16* __restrict__ x, const u16* __restrict__ Wq, const u16* __restrict__ Wk,
                                              const u16* __restrict__ Wv, const u16* __restrict__ Wo, const u16* __restrict__ Wg,
                                              const u16* __restrict__ Wu, const u16* __restrict__ Wd, char* __restrict__ ws) {
    if (blockIdx.x < PREP_LN_BLOCKS) { ln_row(x, (u16*)(ws + OFF_XN), blockIdx.x * 4 + (threadIdx.x >> 6), threadIdx.x & 63); return; }
    if (blockIdx.x < PREP_LN_BLOCKS + PREP_TILE_BLOCKS) {
        const int c = (blockIdx.x - PREP_LN_BLOCKS) * 256 + threadIdx.x;
        if (c < TILE_A) tile_w_block(c, Wq, Wk, Wv, Wo, Wg, Wu, Wd, ws, false);
        return;
    }
    v4u* z = (v4u*)ws;
    for (size_t e = (size_t)(blockIdx.x - PREP_LN_BLOCKS - PREP_TILE_BLOCKS) * 256 + threadIdx.x; e < ZERO_A / 16; e += (size_t)PREP_ZERO_BLOCKS * 256)
        z[e] = (v4u){0u, 0u, 0u, 0u};
}

#define GLDS16(gp, lp) __builtin_amdgcn_global_load_lds((const unsigned*)(gp), (unsigned*)(lp), 16, 0, 0)
constexpr unsigned QKV_MAT_BYTES = (unsigned)S_ * NH * DH * 2;
__device__ __forceinline__ void qkv_tile(char* lds, const bf16x8* __restrict__ XN, const bf16x8* __restrict__ WT, __amdgpu_buffer_rsrc_t qrs,
                                         int mtile, int m, int h, int w, int lane) {
    bf16x8 (*sm)[32][64] = (bf16x8 (*)[32][64])lds;
    const int wm = w >> 1, wn = w & 1, g = lane >> 4, i = lane & 15;
    const bf16x8* sp = w < 2 ? XN + ((size_t)(mtile * 8 + w * 4) * 16) * 64 + lane
                             : WT + ((size_t)(h * 4 + (w - 2) * 2) * 96 + m * 32) * 64 + lane;
    auto stage = [&](int c, int buf) {
#pragma unroll
        for (int f = 0; f < 8; ++f) {
            const int t = f >> 1, kk = 2 * c + (f & 1);
            const int off = w < 2 ? (t * 16 + kk) * 64 : ((t >> 1) * 96 + (t & 1) * 16 + kk) * 64;
            GLDS16(sp + off, &sm[buf][w * 8 + f][0]);
        }
    };
    f32x4 acc[4][4];
#pragma unroll
    for (int a = 0; a < 4; ++a)
#pragma unroll
        for (int b = 0; b < 4; ++b) acc[a][b] = (f32x4){0.f, 0.f, 0.f, 0.f};
    __syncthreads();
    stage(0, 0);
    for (int c = 0; c < 8; ++c) {
        asm volatile("s_waitcnt vmcnt(0)" ::: "memory");
        __syncthreads();
        if (c + 1 < 8) stage(c + 1, (c + 1) & 1);
#pragma unroll
        for (int k2 = 0; k2 < 2; ++k2) {
            bf16x8 a[4], b[4];
#pragma unroll
            for (int t = 0; t < 4; ++t) { a[t] = sm[c & 1][(wm * 4 + t) * 2 + k2][lane]; b[t] = sm[c & 1][16 + (wn * 4 + t) * 2 + k2][lane]; }
            if (m < 2) {
#pragma unroll
                for (int mt = 0; mt < 4; ++mt)
#pragma unroll
                    for (int t = 0; t < 4; ++t) acc[mt][t] = MFMA(b[t], a[mt], acc[mt][t]);
            } else {
#pragma unroll
                for (int mt = 0; mt < 4; ++mt)
#pragma unroll
                    for (int t = 0; t < 4; ++t) acc[mt][t] = MFMA(a[mt], b[t], acc[mt][t]);
            }
        }
    }
    const int mb = mtile * 2 + wm;
    if (m < 2) {
#pragma unroll
        for (int mt = 0; mt < 4; ++mt) {
            const int tok = mb * 64 + mt * 16 + i, s = tok >> 5, tl = tok & 31;
            const int tile = m == 0 ? (tl >> 4) : ((tl >> 2) & 1), row = m == 0 ? (tl & 15) : (4 * (tl >> 3) + (tl & 3));
#pragma unroll
            for (int p = 0; p < 2; ++p) {
                const int ks = 2 * wn + p;
                v4u o = {pack2(acc[mt][2 * p][0], acc[mt][2 * p][1]), pack2(acc[mt][2 * p][2], acc[mt][2 * p][3]),
                         pack2(acc[mt][2 * p + 1][0], acc[mt][2 * p + 1][1]), pack2(acc[mt][2 * p + 1][2], acc[mt][2 * p + 1][3])};
                __builtin_amdgcn_raw_buffer_store_b128(o, qrs, (unsigned)m * QKV_MAT_BYTES + (unsigned)((s * NH + h) * 8192 + ((tile * 4 + ks) * 64 + g * 16 + row) * 16), 0, 16);
            }
        }
    } else {
#pragma unroll
        for (int mt = 0; mt < 4; ++mt) {
            const int s = mb * 2 + (mt >> 1), gp = 2 * (mt & 1) + (g >> 1);
#pragma unroll
            for (int t = 0; t < 4; ++t) {
                const int tau = 4 * wn + t;
                v2u o = {pack2(acc[mt][t][0], acc[mt][t][1]), pack2(acc[mt][t][2], acc[mt][t][3])};
                __builtin_amdgcn_raw_buffer_store_b64(o, qrs, 2u * QKV_MAT_BYTES + (unsigned)((s * NH + h) * 8192 + (tau * 64 + gp * 16 + i) * 16 + (g & 1) * 8), 0, 16);
            }
        }
    }
}

__device__ __forceinline__ void glu_block(const bf16x8* __restrict__ XN, const bf16x8* __restrict__ WG, const bf16x8* __restrict__ WU,
                                          const bf16x8* __restrict__ WD, u16* __restrict__ Bout, char* lds, int mb, int w, int lane) {
    const int g = lane >> 4, i = lane & 15;
    bf16x8* Hl = (bf16x8*)lds;
    const bf16x8* xa = XN + (size_t)(mb * 2) * 16 * 64 + lane;
    for (int fp = w; fp < 56; fp += 4) {
        f32x4 ag[2][2], au[2][2];
#pragma unroll
        for (int a = 0; a < 2; ++a)
#pragma unroll
            for (int b = 0; b < 2; ++b) { ag[a][b] = (f32x4){0.f, 0.f, 0.f, 0.f}; au[a][b] = (f32x4){0.f, 0.f, 0.f, 0.f}; }
        const bf16x8* wg = WG + (size_t)(fp * 2) * 16 * 64 + lane;
        const bf16x8* wu = WU + (size_t)(fp * 2) * 16 * 64 + lane;
#pragma unroll 4
        for (int kk = 0; kk < 16; ++kk) {
            bf16x8 a[2], bg[2], bu[2];
#pragma unroll
            for (int t = 0; t < 2; ++t) { a[t] = xa[(t * 16 + kk) * 64]; bg[t] = wg[(t * 16 + kk) * 64]; bu[t] = wu[(t * 16 + kk) * 64]; }
#pragma unroll
            for (int mt = 0; mt < 2; ++mt)
#pragma unroll
                for (int t = 0; t < 2; ++t) { ag[mt][t] = MFMA(bg[t], a[mt], ag[mt][t]); au[mt][t] = MFMA(bu[t], a[mt], au[mt][t]); }
        }
#pragma unroll
        for (int mt = 0; mt < 2; ++mt) {
            float hv[8];
#pragma unroll
            for (int t = 0; t < 2; ++t)
#pragma unroll
                for (int r = 0; r < 4; ++r) {
                    const float gb = rbf(ag[mt][t][r]), ub = rbf(au[mt][t][r]);
                    const float sg = rbf(1.0f / (1.0f + __expf(-gb)));
                    hv[t * 4 + r] = sg * ub;
                }
            Hl[(mt * 56 + fp) * 64 + lane] = mk8(pack2(hv[0], hv[1]), pack2(hv[2], hv[3]), pack2(hv[4], hv[5]), pack2(hv[6], hv[7]));
        }
    }
    __syncthreads();
    for (int nq = w; nq < 8; nq += 4) {
        f32x4 ab[2][4];
#pragma unroll
        for (int a = 0; a < 2; ++a)
#pragma unroll
            for (int b = 0; b < 4; ++b) ab[a][b] = (f32x4){0.f, 0.f, 0.f, 0.f};
        const bf16x8* pb = WD + (size_t)(nq * 4) * 56 * 64 + lane;
#pragma unroll 4
        for (int kk = 0; kk < 56; ++kk) {
            const bf16x8 a0 = Hl[kk * 64 + lane], a1 = Hl[(56 + kk) * 64 + lane];
#pragma unroll
            for (int t = 0; t < 4; ++t) {
                const bf16x8 b = pb[(t * 56 + kk) * 64];
                ab[0][t] = MFMA(b, a0, ab[0][t]); ab[1][t] = MFMA(b, a1, ab[1][t]);
            }
        }
#pragma unroll
        for (int mt = 0; mt < 2; ++mt)
#pragma unroll
            for (int t = 0; t < 4; ++t)
                *(v2u*)(Bout + (size_t)(mb * 32 + mt * 16 + i) * DM + (nq * 4 + t) * 16 + 4 * g) =
                    (v2u){pack2(ab[mt][t][0], ab[mt][t][1]), pack2(ab[mt][t][2], ab[mt][t][3])};
    }
}

constexpr int L_WO = 0, L_MEM = 131072, L_SC = L_MEM + 8192, L_OE = L_SC + 6144, L_PX = L_OE + 2048, L_TOTAL = L_PX + 4 * 640;
constexpr unsigned SPINCAP = 1u << 20;
#define JS 4
constexpr int NCH = NH * JS;
constexpr int NWORK = 256 - NCH;
constexpr int TPW = 8 / JS;
constexpr float SCALE = 0.08838834764831845f;
#define SLP1 10
#define AUXS 16
#define AUXL 16
#define SLPN 16
#define SLP2 15

__device__ __forceinline__ float xor32_max(float x) { auto r = __builtin_amdgcn_permlane32_swap(__float_as_uint(x), __float_as_uint(x), false, false); return fmaxf(__uint_as_float(r[0]), __uint_as_float(r[1])); }
__device__ __forceinline__ float xor16_max(float x) { auto r = __builtin_amdgcn_permlane16_swap(__float_as_uint(x), __float_as_uint(x), false, false); return fmaxf(__uint_as_float(r[0]), __uint_as_float(r[1])); }
__device__ __forceinline__ float xor32_add(float x) { auto r = __builtin_amdgcn_permlane32_swap(__float_as_uint(x), __float_as_uint(x), false, false); return __uint_as_float(r[0]) + __uint_as_float(r[1]); }
__device__ __forceinline__ float xor16_add(float x) { auto r = __builtin_amdgcn_permlane16_swap(__float_as_uint(x), __float_as_uint(x), false, false); return __uint_as_float(r[0]) + __uint_as_float(r[1]); }
template <int CTRL> __device__ __forceinline__ float dpp_f(float x) { return __int_as_float(__builtin_amdgcn_update_dpp(0, __float_as_int(x), CTRL, 0xf, 0xf, false)); }
__device__ __forceinline__ float ror8(float x) { return __int_as_float(__builtin_amdgcn_update_dpp(0, __float_as_int(x), 0x128, 0xf, 0xf, false)); }

typedef __attribute__((ext_vector_type(2))) float f32x2;
__device__ __forceinline__ unsigned cvtpk(float a, float b) { unsigned r; asm("v_cvt_pk_bf16_f32 %0, %1, %2" : "=v"(r) : "v"(a), "v"(b)); return r; }
__device__ __forceinline__ float vmax(float a, float b) { float r; asm("v_max_f32 %0, %1, %2" : "=v"(r) : "v"(a), "v"(b)); return r; }
__device__ __forceinline__ float vmax3(float a, float b, float c) { float r; asm("v_max3_f32 %0, %1, %2, %3" : "=v"(r) : "v"(a), "v"(b), "v"(c)); return r; }
__device__ __forceinline__ float xmax32(float x) { auto r = __builtin_amdgcn_permlane32_swap(__float_as_uint(x), __float_as_uint(x), false, false); return vmax(__uint_as_float(r[0]), __uint_as_float(r[1])); }
__device__ __forceinline__ float xmax16(float x) { auto r = __builtin_amdgcn_permlane16_swap(__float_as_uint(x), __float_as_uint(x), false, false); return vmax(__uint_as_float(r[0]), __uint_as_float(r[1])); }
constexpr unsigned KM_BYTES = (unsigned)NSEG * NH * MT * DH * 2;
__device__ __forceinline__ void xattn_unit(__amdgpu_buffer_rsrc_t qrs, __amdgpu_buffer_rsrc_t krs, bf16x8* dst, int s, int h, int lane) {
    const int g = lane >> 4, i = lane & 15;
    const unsigned qo = (unsigned)((s * NH + h) * 8192 + lane * 16), mo = (unsigned)((s * NH + h) * 2048);
#define LDQ(off) __builtin_bit_cast(bf16x8, __builtin_amdgcn_raw_buffer_load_b128(qrs, (off), 0, 0))
    bf16x8 kf[4], k1[4], k2[4], qf[2][4], vx[8]; v2u vm[8];
#pragma unroll
    for (int ks = 0; ks < 4; ++ks) {
        kf[ks] = __builtin_bit_cast(bf16x8, __builtin_amdgcn_raw_buffer_load_b128(krs, mo + (unsigned)((i & 7) * 256 + 64 * ks + 16 * g), 0, 0));
        k1[ks] = LDQ(QKV_MAT_BYTES + qo + ks * 1024);
        k2[ks] = LDQ(QKV_MAT_BYTES + qo + (4 + ks) * 1024);
        qf[0][ks] = LDQ(qo + ks * 1024);
        qf[1][ks] = LDQ(qo + (4 + ks) * 1024);
    }
#pragma unroll
    for (int tau = 0; tau < 8; ++tau) {
        vx[tau] = LDQ(2u * QKV_MAT_BYTES + qo + tau * 1024);
        vm[tau] = __builtin_amdgcn_raw_buffer_load_b64(krs, KM_BYTES + mo + (unsigned)((tau * 32 + (lane & 31)) * 8), 0, 0);
    }
#pragma unroll
    for (int qt = 0; qt < 2; ++qt) {
        f32x4 sc[3];
#pragma unroll
        for (int a = 0; a < 3; ++a) sc[a] = (f32x4){0.f, 0.f, 0.f, 0.f};
#pragma unroll
        for (int ks = 0; ks < 4; ++ks) {
            sc[0] = MFMA(kf[ks], qf[qt][ks], sc[0]);
            sc[1] = MFMA(k1[ks], qf[qt][ks], sc[1]);
            sc[2] = MFMA(k2[ks], qf[qt][ks], sc[2]);
        }
        const int tq = qt * 16 + i;
        float sv[12]; bool ok[12];
#pragma unroll
        for (int r = 0; r < 4; ++r) { ok[r] = g < 2; ok[4 + r] = (8 * g + r) <= tq; ok[8 + r] = (8 * g + 4 + r) <= tq; }
        float mx = -3.0e38f;
#pragma unroll
        for (int a = 0; a < 3; ++a)
#pragma unroll
            for (int r = 0; r < 4; ++r) { sv[a * 4 + r] = rbf(rbf(sc[a][r]) * SCALE); mx = ok[a * 4 + r] ? fmaxf(mx, sv[a * 4 + r]) : mx; }
        mx = xor16_max(mx);
        mx = xor32_max(mx);
        float ex[12], sum = 0.f;
#pragma unroll
        for (int a = 0; a < 12; ++a) { ex[a] = ok[a] ? __expf(sv[a] - mx) : 0.f; sum += ex[a]; }
        sum = xor16_add(sum);
        sum = xor32_add(sum);
        const float inv = 1.0f / sum;
        const bf16x8 pX = mk8(pack2(ex[4] * inv, ex[5] * inv), pack2(ex[6] * inv, ex[7] * inv), pack2(ex[8] * inv, ex[9] * inv), pack2(ex[10] * inv, ex[11] * inv));
        const bf16x8 pM = mk8(pack2(ex[0] * inv, ex[1] * inv), pack2(ex[2] * inv, ex[3] * inv), 0u, 0u);
#pragma unroll
        for (int ks = 0; ks < 4; ++ks) {
            f32x4 o[2];
#pragma unroll
            for (int t = 0; t < 2; ++t) {
                const int tau = 2 * ks + t;
                o[t] = MFMA(vx[tau], pX, ((f32x4){0.f, 0.f, 0.f, 0.f}));
                o[t] = MFMA(mk8(vm[tau].x, vm[tau].y, 0u, 0u), pM, o[t]);
            }
            dst[(qt * 16 + ks) * 64 + lane] =
                mk8(pack2(o[0][0], o[0][1]), pack2(o[0][2], o[0][3]), pack2(o[1][0], o[1][1]), pack2(o[1][2], o[1][3]));
        }
    }
}

#define AMFMA0(d, a, b) asm volatile("s_nop 1\n\tv_mfma_f32_16x16x32_bf16 %0, %1, %2, 0" : "=&v"(d) : "v"(a), "v"(b))
#define AMFMA(d, a, b) asm volatile("s_nop 1\n\tv_mfma_f32_16x16x32_bf16 %0, %1, %2, %0" : "+v"(d) : "v"(a), "v"(b))
#define AMFMA16(d, a, b) asm volatile("s_nop 1\n\tv_mfma_f32_16x16x16_bf16 %0, %1, %2, %0" : "+v"(d) : "v"(a), "v"(b))

__global__ __launch_bounds__(256, 1) void k_chain(const u16* __restrict__ mem0, const bf16x8* __restrict__ WT, const bf16x8* __restrict__ WOT,
                                                  u16* __restrict__ KM, u16* __restrict__ VM, unsigned* Pbuf, unsigned* Mbuf, unsigned* tmo,
                                                  const bf16x8* __restrict__ XN, const bf16x8* __restrict__ WG, const bf16x8* __restrict__ WU,
                                                  const bf16x8* __restrict__ WD, u16* __restrict__ QX, u16* __restrict__ Bout,
                                                  const u16* __restrict__ Wg, const u16* __restrict__ Wu, const u16* __restrict__ Wd, char* ws) {
    extern __shared__ __attribute__((aligned(16))) char lds[];
    const int h = blockIdx.x & 31, jj = blockIdx.x >> 5;
    const int w = __builtin_amdgcn_readfirstlane(threadIdx.x >> 6), lane = threadIdx.x & 63, g = lane >> 4, i = lane & 15;
    const __amdgpu_buffer_rsrc_t qrs = __builtin_amdgcn_make_buffer_rsrc(QX, 0, 3u * QKV_MAT_BYTES, 0x00020000);
    const __amdgpu_buffer_rsrc_t krs = __builtin_amdgcn_make_buffer_rsrc(KM, 0, 2u * KM_BYTES, 0x00020000);
    unsigned* kvcnt = tmo + 16;
    unsigned* zcnt = tmo + 64;
    if (blockIdx.x >= NCH) {
        const int wb = blockIdx.x - NCH;
        for (int t = wb; t < 1024; t += NWORK) {
            qkv_tile(lds, XN, WT, qrs, t >> 6, 1 + ((t >> 5) & 1), t & 31, w, lane);
            asm volatile("s_waitcnt vmcnt(0)" ::: "memory");
            __syncthreads();
            if (threadIdx.x == 0) __hip_atomic_fetch_add(kvcnt + (t >> 6), 1u, __ATOMIC_RELAXED, __HIP_MEMORY_SCOPE_AGENT);
            if (t == wb)
            {
                const __amdgpu_buffer_rsrc_t zrs = __builtin_amdgcn_make_buffer_rsrc(Pbuf, 0, 63 * 32 * 16384, 0x00020000);
                for (unsigned e = (unsigned)ZSTEPS * 32768u + (unsigned)(wb * 256 + threadIdx.x); e < 63u * 32768u; e += (unsigned)NWORK * 256u)
                    __builtin_amdgcn_raw_buffer_store_b128((v4u){0u, 0u, 0u, 0u}, zrs, e * 16u, 0, 16);
                for (int c = TILE_A + wb * 256 + threadIdx.x; c < TILE_BLOCKS; c += NWORK * 256) tile_w_block(c, Wg, Wg, Wg, Wg, Wg, Wu, Wd, ws, true);
                asm volatile("s_waitcnt vmcnt(0)" ::: "memory");
                __syncthreads();
                if (threadIdx.x == 0) __hip_atomic_fetch_add(zcnt, 1u, __ATOMIC_RELAXED, __HIP_MEMORY_SCOPE_AGENT);
            }
        }
        __syncthreads();
        if (wb < 64) {
            if (threadIdx.x == 0) {
                unsigned spins = 0;
                while (__hip_atomic_load(zcnt, __ATOMIC_RELAXED, __HIP_MEMORY_SCOPE_AGENT) < (unsigned)NWORK) {
                    __builtin_amdgcn_s_sleep(8);
                    if (++spins > SPINCAP) { atomicAdd(tmo, 1u); break; }
                }
                __builtin_amdgcn_fence(__ATOMIC_ACQUIRE, "agent");
                asm volatile("s_waitcnt vmcnt(0)" ::: "memory");
            }
            __syncthreads();
            glu_block(XN, WG, WU, WD, Bout, lds, wb, w, lane);
        } else {
            for (int t = wb - 64; t < 512; t += NWORK - 64) {
                qkv_tile(lds, XN, WT, qrs, t >> 5, 0, t & 31, w, lane);
            }
        }
        return;
    }
    unsigned kv_ready = 0;
    auto wait_kv = [&](int grp) {
        unsigned spins = 0;
        while (!((kv_ready >> grp) & 1u)) {
            const unsigned c = __hip_atomic_load(kvcnt + (lane & 15), __ATOMIC_RELAXED, __HIP_MEMORY_SCOPE_AGENT);
            kv_ready = (unsigned)__ballot(c >= 64u) & 0xffffu;
            if ((kv_ready >> grp) & 1u) break;
            __builtin_amdgcn_s_sleep(2);
            if (++spins > SPINCAP) { if (lane == 0) atomicAdd(tmo, 1u); break; }
        }
    };
    auto fetch_kv = [&](int sg, v4u (&pk)[2], v4u (&pv)[2]) {
        const unsigned o = (unsigned)((sg * NH + h) * 8192 + lane * 16);
        pk[0] = __builtin_amdgcn_raw_buffer_load_b128(qrs, QKV_MAT_BYTES + o + w * 1024, 0, 16);
        pk[1] = __builtin_amdgcn_raw_buffer_load_b128(qrs, QKV_MAT_BYTES + o + w * 1024 + 4096, 0, 16);
        pv[0] = __builtin_amdgcn_raw_buffer_load_b128(qrs, 2u * QKV_MAT_BYTES + o + w * 2048, 0, 16);
        pv[1] = __builtin_amdgcn_raw_buffer_load_b128(qrs, 2u * QKV_MAT_BYTES + o + w * 2048 + 1024, 0, 16);
    };
    const __amdgpu_buffer_rsrc_t prs = __builtin_amdgcn_make_buffer_rsrc(Pbuf, 0, 63 * 32 * 16384, 0x00020000);
    const __amdgpu_buffer_rsrc_t mrs = __builtin_amdgcn_make_buffer_rsrc(Mbuf, 0, 64 * 8192, 0x00020000);

    bf16x8 WA[64], WV[32];
    {
        const bf16x8* src = WT + (size_t)(h * 4 + w) * 96 * 64 + lane;
#pragma unroll
        for (int f = 0; f < 64; ++f) WA[f] = src[f * 64];
#pragma unroll
        for (int f = 0; f < 32; ++f) WV[f] = src[(64 + f) * 64];
        const bf16x8* so = WOT + ((size_t)h * 128 + (jj * 4 + w) * TPW * 4) * 64 + lane;
        bf16x8* lo = (bf16x8*)(lds + L_WO) + (w * TPW * 4) * 64 + lane;
#pragma unroll 8
        for (int f = 0; f < TPW * 4; ++f) lo[f * 64] = so[f * 64];
    }
    {
        const int tid = threadIdx.x;
#pragma unroll
        for (int c = tid; c < 512; c += 256) {
            const int t = c & 7, gg = (c >> 3) & 3, kk = c >> 5;
            *(v4u*)(lds + L_MEM + c * 16) = *(const v4u*)(mem0 + t * DM + 32 * kk + 8 * gg);
        }
    }
    v4u pk[2], pv[2];
    wait_kv(0);
    fetch_kv(0, pk, pv);
    const int rid = blockIdx.x * 4 + w;
    const bool is_red = (rid % JS) == 0;
    const int red = rid / JS;
#define STAMP(k)
    for (int s = 0; s < NSEG; ++s) {
        __syncthreads();
        STAMP(0);
        asm volatile("" : "+v"(pk[0]), "+v"(pk[1]), "+v"(pv[0]), "+v"(pv[1]));
        f32x4 acc[6];
        bf16x8 mf[16];
        const char* mp = lds + L_MEM + g * 128 + (i & 7) * 16;
        {
#pragma unroll
            for (int kk = 0; kk < 4; ++kk) mf[kk] = *(const bf16x8*)(mp + kk * 512);
#pragma unroll
            for (int kk = 0; kk < 16; ++kk) {
                if (kk + 4 < 16) mf[kk + 4] = *(const bf16x8*)(mp + (kk + 4) * 512);
                if (kk == 0) {
                    asm volatile("v_mfma_f32_16x16x32_bf16 %0, %4, %8, 0\n\tv_mfma_f32_16x16x32_bf16 %1, %5, %8, 0\n\t"
                                 "v_mfma_f32_16x16x32_bf16 %2, %6, %8, 0\n\tv_mfma_f32_16x16x32_bf16 %3, %7, %8, 0"
                                 : "=&v"(acc[0]), "=&v"(acc[1]), "=&v"(acc[2]), "=&v"(acc[3])
                                 : "a"(WA[0]), "a"(WA[16]), "a"(WA[32]), "a"(WA[48]), "v"(mf[0]));
                } else {
                    asm volatile("v_mfma_f32_16x16x32_bf16 %0, %4, %8, %0\n\tv_mfma_f32_16x16x32_bf16 %1, %5, %8, %1\n\t"
                                 "v_mfma_f32_16x16x32_bf16 %2, %6, %8, %2\n\tv_mfma_f32_16x16x32_bf16 %3, %7, %8, %3"
                                 : "+v"(acc[0]), "+v"(acc[1]), "+v"(acc[2]), "+v"(acc[3])
                                 : "a"(WA[kk]), "a"(WA[16 + kk]), "a"(WA[32 + kk]), "a"(WA[48 + kk]), "v"(mf[kk]));
                }
            }
            asm volatile("s_nop 7\n\ts_nop 1" : "+v"(acc[0]), "+v"(acc[1]), "+v"(acc[2]), "+v"(acc[3]));
        }
        const bf16x8 qf = mk8(pack2(acc[0][0], acc[0][1]), pack2(acc[0][2], acc[0][3]), pack2(acc[1][0], acc[1][1]), pack2(acc[1][2], acc[1][3]));
        const bf16x8 kf = mk8(pack2(acc[2][0], acc[2][1]), pack2(acc[2][2], acc[2][3]), pack2(acc[3][0], acc[3][1]), pack2(acc[3][2], acc[3][3]));
        if (i < 8 && jj == 0) __builtin_amdgcn_raw_buffer_store_b128(__builtin_bit_cast(v4u, kf), krs, (unsigned)((s * NH + h) * 2048 + i * 256 + 64 * w + 16 * g), 0, 0);
        {
            f32x4 sp[3];
            AMFMA0(sp[0], kf, qf); AMFMA0(sp[1], pk[0], qf); AMFMA0(sp[2], pk[1], qf);
            asm volatile("s_nop 7" : "+v"(sp[0]), "+v"(sp[1]), "+v"(sp[2]));
            if (i < 8) {
#pragma unroll
                for (int a = 0; a < 3; ++a) *(f32x4*)(lds + L_SC + (((w * 3 + a) * 4 + g) * 8 + i) * 16) = sp[a];
            }
        }
#pragma unroll
        for (int kk = 0; kk < 4; ++kk) mf[kk] = *(const bf16x8*)(mp + kk * 512);
        STAMP(1);
        __syncthreads();
        STAMP(2);
#define VS(kk, hf)                                                                                                             \
        do {                                                                                                                   \
            if ((hf) == 0 && (kk) + 4 < 16) mf[((kk) + 4) & 15] = *(const bf16x8*)(mp + ((kk) + 4) * 512);                     \
            if ((kk) == 0) asm volatile("v_mfma_f32_16x16x32_bf16 %0, %2, %1, 0" : "=&v"(acc[4 + (hf)]) : "v"(WV[16 * (hf)]), "v"(mf[0]));      \
            else asm volatile("v_mfma_f32_16x16x32_bf16 %0, %2, %1, %0" : "+v"(acc[4 + (hf)]) : "v"(WV[16 * (hf) + (kk)]), "v"(mf[kk]));        \
        } while (0)
        v2u v0, v1;
        bf16x8 wf[4][8];
        const char* wp = lds + L_WO + (w * TPW * 4) * 1024 + lane * 16;
        {
            const int q = lane & 7, kg = lane >> 3;
            const char* sx = lds + L_SC + (((1 + (kg & 1)) * 4 + (kg >> 1)) * 8 + q) * 16;
            const char* sm_ = lds + L_SC + ((kg >> 2) * 8 + q) * 16 + (kg & 3) * 4;
            f32x4 px[4]; float pm[4];
#pragma unroll
            for (int ww = 0; ww < 4; ++ww) { px[ww] = *(const f32x4*)(sx + ww * 1536); pm[ww] = *(const float*)(sm_ + ww * 1536); }
            VS(0, 0); VS(0, 1); VS(1, 0); VS(1, 1); VS(2, 0); VS(2, 1); VS(3, 0); VS(3, 1);
            const f32x4 tx = (px[0] + px[1]) + (px[2] + px[3]);
            VS(4, 0);
            const float tm = (pm[0] + pm[1]) + (pm[2] + pm[3]);
            VS(4, 1);
            const unsigned u0 = cvtpk(tx[0], tx[1]), u1 = cvtpk(tx[2], tx[3]), u2 = cvtpk(tm, tm);
            VS(5, 0);
            const f32x4 bx = (f32x4){__uint_as_float(u0 << 16), __uint_as_float(u0 & 0xffff0000u), __uint_as_float(u1 << 16), __uint_as_float(u1 & 0xffff0000u)} * SCALE;
            const float bm = __uint_as_float(u2 << 16) * SCALE;
            VS(5, 1);
            const unsigned w0 = cvtpk(bx[0], bx[1]), w1 = cvtpk(bx[2], bx[3]), w2 = cvtpk(bm, bm);
            VS(6, 0);
            const float s0 = __uint_as_float(w0 << 16), s1 = __uint_as_float(w0 & 0xffff0000u), s2 = __uint_as_float(w1 << 16),
                        s3 = __uint_as_float(w1 & 0xffff0000u), s4 = __uint_as_float(w2 << 16);
            VS(6, 1);
            float mx = vmax3(vmax3(s0, s1, s2), s3, s4);
            VS(7, 0);
            mx = vmax(mx, ror8(mx)); VS(7, 1);
            mx = xmax16(mx); VS(8, 0);
            mx = xmax32(mx); VS(8, 1);
            constexpr float L2E = 1.4426950408889634f;
            const float nm = -mx * L2E;
            const float e0 = __builtin_amdgcn_exp2f(__builtin_fmaf(s0, L2E, nm)), e1 = __builtin_amdgcn_exp2f(__builtin_fmaf(s1, L2E, nm));
            VS(9, 0);
            const float e2 = __builtin_amdgcn_exp2f(__builtin_fmaf(s2, L2E, nm)), e3 = __builtin_amdgcn_exp2f(__builtin_fmaf(s3, L2E, nm));
            VS(9, 1);
            const float e4 = __builtin_amdgcn_exp2f(__builtin_fmaf(s4, L2E, nm));
            float sum = ((e0 + e1) + (e2 + e3)) + e4;
            VS(10, 0);
            sum += ror8(sum); VS(10, 1);
            sum = xor16_add(sum); VS(11, 0);
            sum = xor32_add(sum); VS(11, 1);
            const float inv = __builtin_amdgcn_rcpf(sum);
            char* pw = lds + L_PX + w * 640;
            *(v2u*)(pw + q * 64 + kg * 8) = (v2u){cvtpk(e0 * inv, e1 * inv), cvtpk(e2 * inv, e3 * inv)};
            *(u16*)(pw + 512 + q * 16 + kg * 2) = (u16)cvtpk(e4 * inv, e4 * inv);
            VS(12, 0); VS(12, 1); VS(13, 0); VS(13, 1);
            __builtin_amdgcn_wave_barrier();
            const bf16x8 pX = *(const bf16x8*)(pw + (i & 7) * 64 + g * 16);
            v2u pM = *(const v2u*)(pw + 512 + (i & 7) * 16 + (g & 1) * 8);
#pragma unroll
            for (int f = 0; f < 8; ++f) wf[0][f] = *(const bf16x8*)(wp + f * 1024);
            VS(14, 0); VS(14, 1); VS(15, 0); VS(15, 1);
            asm volatile("s_nop 7" : "+v"(acc[4]), "+v"(acc[5]));
            v0 = (v2u){cvtpk(acc[4][0], acc[4][1]), cvtpk(acc[4][2], acc[4][3])};
            v1 = (v2u){cvtpk(acc[5][0], acc[5][1]), cvtpk(acc[5][2], acc[5][3])};
            if (g >= 2) pM = (v2u){0u, 0u};
            f32x4 o0, o1;
            AMFMA0(o0, pv[0], pX); AMFMA0(o1, pv[1], pX);
            asm volatile("s_nop 5" : "+v"(o0), "+v"(o1));
            AMFMA16(o0, v0, pM); AMFMA16(o1, v1, pM);
            asm volatile("s_nop 7" : "+v"(o0), "+v"(o1));
            if (i < 8) *(bf16x8*)(lds + L_OE + ((w * 4 + g) * 8 + i) * 16) = mk8(cvtpk(o0[0], o0[1]), cvtpk(o0[2], o0[3]), cvtpk(o1[0], o1[1]), cvtpk(o1[2], o1[3]));
        }
        if (g < 2 && jj == 0) {
            __builtin_amdgcn_raw_buffer_store_b64(v0, krs, KM_BYTES + (unsigned)((s * NH + h) * 2048 + (2 * w * 32 + lane) * 8), 0, 0);
            __builtin_amdgcn_raw_buffer_store_b64(v1, krs, KM_BYTES + (unsigned)((s * NH + h) * 2048 + ((2 * w + 1) * 32 + lane) * 8), 0, 0);
        }
        if (s == NSEG - 1) break;
        const unsigned pofs = (unsigned)s * (32u * 16384u);
        if (s == ZSTEPS) {
            unsigned spins = 0;
            while ((unsigned)__builtin_amdgcn_readfirstlane(__hip_atomic_load(zcnt, __ATOMIC_RELAXED, __HIP_MEMORY_SCOPE_AGENT)) < (unsigned)NWORK) {
                __builtin_amdgcn_s_sleep(2);
                if (++spins > SPINCAP) { if (lane == 0) atomicAdd(tmo, 1u); break; }
            }
        }
        {
            __syncthreads();
            STAMP(3);
            bf16x8 of[4];
#pragma unroll
            for (int ks = 0; ks < 4; ++ks) of[ks] = *(const bf16x8*)(lds + L_OE + ((ks * 4 + g) * 8 + (i & 7)) * 16);
#pragma unroll
            for (int a = 0; a < TPW / 2; ++a) {
                if (a + 1 < TPW / 2) {
#pragma unroll
                    for (int f = 0; f < 8; ++f) wf[a + 1][f] = *(const bf16x8*)(wp + ((a + 1) * 8 + f) * 1024);
                }
                f32x4 d0, d1;
                AMFMA0(d0, wf[a][0], of[0]); AMFMA0(d1, wf[a][4], of[0]);
#pragma unroll
                for (int ks = 1; ks < 4; ++ks) { AMFMA(d0, wf[a][ks], of[ks]); AMFMA(d1, wf[a][4 + ks], of[ks]); }
                asm volatile("s_nop 7" : "+v"(d0), "+v"(d1));
                v4u v;
#pragma unroll
                for (int r = 0; r < 4; ++r) {
                    const float x1 = ror8(d1[r]);
                    v[r] = __float_as_uint(i < 8 ? d0[r] : x1) | 1u;
                }
                const int T = (jj * 4 + w) * TPW + 2 * a + (i >> 3);
                __builtin_amdgcn_raw_buffer_store_b128(v, prs, pofs + (unsigned)(h * 16384 + (4 * T + g) * 128 + (i & 7) * 16), 0, AUXS);
            }
        }
        STAMP(4);
        if (((s + 1) & 3) == 0) wait_kv((s + 1) >> 2);
        fetch_kv(s + 1, pk, pv);
        for (int z = 0; z < SLP1; ++z) __builtin_amdgcn_s_sleep(1);
        if (!is_red) {
            for (int z = 0; z < SLPN; ++z) __builtin_amdgcn_s_sleep(1);
        } else {
            v4u r[4];
            unsigned spins = 0;
            for (;;) {
#pragma unroll
                for (int q = 0; q < 4; ++q)
                    r[q] = __builtin_amdgcn_raw_buffer_load_b128(prs, pofs + (unsigned)((q * 8 + (lane >> 3)) * 16384 + red * 128 + (lane & 7) * 16), 0, AUXL);
                bool ok = true;
#pragma unroll
                for (int q = 0; q < 4; ++q) ok &= (r[q].x & r[q].y & r[q].z & r[q].w & 1u) != 0u;
                if (__all(ok)) break;
                if (++spins > SPINCAP) { if (lane == 0) atomicAdd(tmo, 1u); break; }
            }
            STAMP(5);
            float sm[4];
#pragma unroll
            for (int c = 0; c < 4; ++c) {
                sm[c] = (__uint_as_float(r[0][c] & ~1u) + __uint_as_float(r[1][c] & ~1u)) + (__uint_as_float(r[2][c] & ~1u) + __uint_as_float(r[3][c] & ~1u));
                sm[c] += ror8(sm[c]);
                sm[c] = xor16_add(sm[c]);
                sm[c] = xor32_add(sm[c]);
            }
            if (lane < 8) {
                v2u v = {pack2(sm[0], sm[1]), pack2(sm[2], sm[3])};
                if (v.x == 0u) v.x = 0x8000u;
                if (v.y == 0u) v.y = 0x8000u;
                __builtin_amdgcn_raw_buffer_store_b64(v, mrs, (unsigned)(s * 8192 + red * 64 + lane * 8), 0, AUXS);
            }
        }
        STAMP(6);
        for (int z = 0; z < SLP2; ++z) __builtin_amdgcn_s_sleep(1);
        {
            v4u m[2];
            unsigned spins = 0;
            for (;;) {
#pragma unroll
                for (int q = 0; q < 2; ++q) m[q] = __builtin_amdgcn_raw_buffer_load_b128(mrs, (unsigned)(s * 8192 + w * 2048 + q * 1024 + lane * 16), 0, AUXL);
                bool ok = true;
#pragma unroll
                for (int q = 0; q < 2; ++q) ok &= (m[q].x != 0u) & (m[q].y != 0u) & (m[q].z != 0u) & (m[q].w != 0u);
                if (__all(ok)) break;
                if (++spins > SPINCAP) { if (lane == 0) atomicAdd(tmo, 1u); break; }
            }
#pragma unroll
            for (int q = 0; q < 2; ++q) {
                const int R = w * 32 + q * 16 + (lane >> 2), t = 2 * (lane & 3);
                char* dst = lds + L_MEM + (R >> 3) * 512 + ((R >> 1) & 3) * 128 + t * 16 + (R & 1) * 8;
                *(v2u*)dst = (v2u){m[q].x, m[q].y};
                *(v2u*)(dst + 16) = (v2u){m[q].z, m[q].w};
            }
        }
        STAMP(7);
    }
}

__global__ __launch_bounds__(512, 2) void k_post(u16* QX, u16* KM, const bf16x8* __restrict__ WOT, f32x4* __restrict__ part) {
    __shared__ bf16x8 sa[4][16][64];
    const __amdgpu_buffer_rsrc_t qrs = __builtin_amdgcn_make_buffer_rsrc(QX, 0, 3u * QKV_MAT_BYTES, 0x00020000);
    const __amdgpu_buffer_rsrc_t krs = __builtin_amdgcn_make_buffer_rsrc(KM, 0, 2u * KM_BYTES, 0x00020000);
    const int w = __builtin_amdgcn_readfirstlane(threadIdx.x >> 6), lane = threadIdx.x & 63;
    const int kq = blockIdx.x & 7, mb = blockIdx.x >> 3;
    const bf16x8* pb = WOT + ((size_t)(kq * 4) * 32 + w * 4) * 4 * 64 + lane;
    bf16x8 b[4][4];
#pragma unroll
    for (int pk = 0; pk < 3; ++pk)
#pragma unroll
        for (int t = 0; t < 4; ++t) b[pk][t] = pb[(t * 4 + pk) * 64];
    xattn_unit(qrs, krs, &sa[(w >> 2) * 2][(w & 3) * 4][0], mb * 2 + (w >> 2), kq * 4 + (w & 3), lane);
    __syncthreads();
    f32x4 acc[4][4];
#pragma unroll
    for (int a = 0; a < 4; ++a)
#pragma unroll
        for (int t = 0; t < 4; ++t) acc[a][t] = (f32x4){0.f, 0.f, 0.f, 0.f};
#pragma unroll
    for (int kk = 0; kk < 16; ++kk) {
        if (kk + 3 < 16) {
            const int k2 = kk + 3;
#pragma unroll
            for (int t = 0; t < 4; ++t) b[k2 & 3][t] = pb[(((k2 >> 2) * 32 + t) * 4 + (k2 & 3)) * 64];
        }
        __builtin_amdgcn_sched_barrier(0);
        bf16x8 a[4];
#pragma unroll
        for (int mt = 0; mt < 4; ++mt) a[mt] = sa[mt][kk][lane];
#pragma unroll
        for (int mt = 0; mt < 4; ++mt)
#pragma unroll
            for (int t = 0; t < 4; ++t) acc[mt][t] = MFMA(b[kk & 3][t], a[mt], acc[mt][t]);
        __builtin_amdgcn_sched_barrier(0);
    }
#pragma unroll
    for (int mt = 0; mt < 4; ++mt)
#pragma unroll
        for (int t = 0; t < 4; ++t) part[(((size_t)kq * 128 + mb * 4 + mt) * 32 + w * 4 + t) * 64 + lane] = acc[mt][t];
}

__global__ __launch_bounds__(256) void k_out(const f32x4* __restrict__ part, const u16* __restrict__ Bout, const u16* __restrict__ x,
                                             u16* __restrict__ out) {
    const int id = blockIdx.x * 256 + threadIdx.x, lane = id & 63, tile = id >> 6, g = lane >> 4, i = lane & 15;
    constexpr size_t KS = (size_t)128 * 32 * 64;
    const f32x4 a = ((part[id] + part[KS + id]) + (part[2 * KS + id] + part[3 * KS + id])) +
                    ((part[4 * KS + id] + part[5 * KS + id]) + (part[6 * KS + id] + part[7 * KS + id]));
    const size_t idx = (size_t)((tile >> 5) * 16 + i) * DM + (tile & 31) * 16 + 4 * g;
    const v2u xr = *(const v2u*)(x + idx), br = *(const v2u*)(Bout + idx);
    const float xv[4] = {__uint_as_float(xr.x << 16), __uint_as_float(xr.x & 0xffff0000u), __uint_as_float(xr.y << 16), __uint_as_float(xr.y & 0xffff0000u)};
    const float bv[4] = {__uint_as_float(br.x << 16), __uint_as_float(br.x & 0xffff0000u), __uint_as_float(br.y << 16), __uint_as_float(br.y & 0xffff0000u)};
    float y[4];
#pragma unroll
    for (int r = 0; r < 4; ++r) y[r] = rbf(xv[r] + rbf(a[r])) + bv[r];
    *(v2u*)(out + idx) = (v2u){pack2(y[0], y[1]), pack2(y[2], y[3])};
}

extern "C" void kernel_launch(void* const* d_in, const int* in_sizes, int n_in, void* d_out, int out_size, void* d_ws, size_t ws_size,
                              hipStream_t stream) {
    const u16* x = (const u16*)d_in[0];
    const u16* mem0 = (const u16*)d_in[1];
    const u16* Wq = (const u16*)d_in[2];
    const u16* Wk = (const u16*)d_in[3];
    const u16* Wv = (const u16*)d_in[4];
    const u16* Wo = (const u16*)d_in[5];
    const u16* Wg = (const u16*)d_in[8];
    const u16* Wu = (const u16*)d_in[9];
    const u16* Wd = (const u16*)d_in[10];
    char* ws = (char*)d_ws;
    (void)hipFuncSetAttribute((const void*)k_chain, hipFuncAttributeMaxDynamicSharedMemorySize, L_TOTAL);
    k_prep<<<PREP_LN_BLOCKS + PREP_TILE_BLOCKS + PREP_ZERO_BLOCKS, 256, 0, stream>>>(x, Wq, Wk, Wv, Wo, Wg, Wu, Wd, ws);
    k_chain<<<NCH + NWORK, 256, L_TOTAL, stream>>>(mem0, (const bf16x8*)(ws + OFF_WT), (const bf16x8*)(ws + OFF_WOT), (u16*)(ws + OFF_KM), (u16*)(ws + OFF_VM), (unsigned*)(ws + OFF_P),
                                                  (unsigned*)(ws + OFF_M), (unsigned*)(ws + OFF_TMO), (const bf16x8*)(ws + OFF_XN),
                                                  (const bf16x8*)(ws + OFF_WG), (const bf16x8*)(ws + OFF_WU), (const bf16x8*)(ws + OFF_WD),
                                                  (u16*)(ws + OFF_QX), (u16*)(ws + OFF_H), Wg, Wu, Wd, ws);
    k_post<<<256, 512, 0, stream>>>((u16*)(ws + OFF_QX), (u16*)(ws + OFF_KM), (const bf16x8*)(ws + OFF_WOT), (f32x4*)(ws + OFF_PART));
    k_out<<<S_ * DM / 4 / 256, 256, 0, stream>>>((const f32x4*)(ws + OFF_PART), (const u16*)(ws + OFF_H), x, (u16*)d_out);
}
```

```cpp
#include <hip/hip_runtime.h>
#include <hip/hip_bf16.h>

constexpr int S_ = 2048, DM = 512, NH = 32, DH = 128, MT = 8, SEG = 32, NSEG = 64, FF = 1792;

typedef unsigned short u16;
typedef __attribute__((ext_vector_type(8))) short bf16x8;
typedef __attribute__((ext_vector_type(4))) float f32x4;
typedef __attribute__((ext_vector_type(4))) unsigned v4u;
typedef __attribute__((ext_vector_type(2))) unsigned v2u;
typedef __bf16 bf16x2_t __attribute__((ext_vector_type(2)));

__device__ __forceinline__ unsigned pack2(float a, float b) {
    bf16x2_t v = {(__bf16)a, (__bf16)b};
    return __builtin_bit_cast(unsigned, v);
}
__device__ __forceinline__ float rbf(float a) { return (float)(__bf16)a; }
__device__ __forceinline__ float bf2f(u16 b) { return __uint_as_float(((unsigned)b) << 16); }
__device__ __forceinline__ bf16x8 mk8(unsigned a, unsigned b, unsigned c, unsigned d) {
    v4u v = {a, b, c, d};
    return __builtin_bit_cast(bf16x8, v);
}
#define MFMA(a, b, c) __builtin_amdgcn_mfma_f32_16x16x32_bf16((a), (b), (c), 0, 0, 0)

__device__ __forceinline__ int pi_col(int tau, int rho) { return 32 * (tau >> 1) + 8 * (rho >> 2) + 4 * (tau & 1) + (rho & 3); }

constexpr size_t OFF_TMO = 0;
constexpr size_t OFF_M   = 65536;
constexpr size_t OFF_P   = OFF_M + 64 * 8192;
constexpr size_t ZERO_BYTES = OFF_P + (size_t)63 * 32 * 16384;
constexpr size_t OFF_XN  = (ZERO_BYTES + 4095) / 4096 * 4096;
constexpr size_t OFF_WT  = OFF_XN + (size_t)S_ * DM * 2;
constexpr size_t OFF_WOT = OFF_WT + (size_t)3 * DM * NH * DH * 2;
constexpr size_t OFF_WG  = OFF_WOT + (size_t)DM * NH * DH * 2;
constexpr size_t OFF_WU  = OFF_WG + (size_t)DM * FF * 2;
constexpr size_t OFF_WD  = OFF_WU + (size_t)DM * FF * 2;
constexpr size_t OFF_QX  = OFF_WD + (size_t)DM * FF * 2;
constexpr size_t OFF_KX  = OFF_QX + (size_t)S_ * NH * DH * 2;
constexpr size_t OFF_VXT = OFF_KX + (size_t)S_ * NH * DH * 2;
constexpr size_t OFF_KM  = OFF_VXT + (size_t)S_ * NH * DH * 2;
constexpr size_t OFF_VM  = OFF_KM + (size_t)NSEG * NH * MT * DH * 2;
constexpr size_t OFF_H   = OFF_VM + (size_t)NSEG * NH * MT * DH * 2;
constexpr size_t OFF_O   = OFF_H + (size_t)S_ * FF * 2;
constexpr size_t OFF_PART = OFF_O + (size_t)S_ * NH * DH * 2;
constexpr size_t WS_TOTAL = OFF_PART + (size_t)8 * S_ * DM * 4;

__device__ __forceinline__ void ln_row(const u16* __restrict__ x, u16* __restrict__ xn_ft, int row, int lane) {
    const v4u raw = *(const v4u*)(x + (size_t)row * DM + lane * 8);
    float v[8];
    v[0] = __uint_as_float(raw.x << 16); v[1] = __uint_as_float(raw.x & 0xffff0000u);
    v[2] = __uint_as_float(raw.y << 16); v[3] = __uint_as_float(raw.y & 0xffff0000u);
    v[4] = __uint_as_float(raw.z << 16); v[5] = __uint_as_float(raw.z & 0xffff0000u);
    v[6] = __uint_as_float(raw.w << 16); v[7] = __uint_as_float(raw.w & 0xffff0000u);
    float s = 0.f;
#pragma unroll
    for (int j = 0; j < 8; ++j) s += v[j];
#pragma unroll
    for (int o = 1; o < 64; o <<= 1) s += __shfl_xor(s, o);
    const float mean = s * (1.0f / DM);
    float q = 0.f;
#pragma unroll
    for (int j = 0; j < 8; ++j) { const float d = v[j] - mean; q += d * d; }
#pragma unroll
    for (int o = 1; o < 64; o <<= 1) q += __shfl_xor(q, o);
    const float mb = rbf(mean), vb = rbf(q * (1.0f / DM));
    const float den = rbf(sqrtf(rbf(vb + 1e-05f)));
    float y[8];
#pragma unroll
    for (int j = 0; j < 8; ++j) y[j] = rbf(v[j] - mb) / den;
    v4u o4 = {pack2(y[0], y[1]), pack2(y[2], y[3]), pack2(y[4], y[5]), pack2(y[6], y[7])};
    const int mt = row >> 4, kk = lane >> 2, fl = (lane & 3) * 16 + (row & 15);
    *(v4u*)(xn_ft + (((size_t)mt * 16 + kk) * 64 + fl) * 8) = o4;
}

__device__ __forceinline__ void transpose8x8(const v4u (&r)[8], v4u (&o)[8]) {
#pragma unroll
    for (int c = 0; c < 4; ++c) {
#pragma unroll
        for (int d = 0; d < 4; ++d) {
            o[2 * c][d] = __builtin_amdgcn_perm(r[2 * d + 1][c], r[2 * d][c], 0x05040100u);
            o[2 * c + 1][d] = __builtin_amdgcn_perm(r[2 * d + 1][c], r[2 * d][c], 0x07060302u);
        }
    }
}
constexpr int B_WT = 3 * 64 * 512, B_WOT = 512 * 64, B_WG = 64 * 224, B_WD = 224 * 64;
constexpr int TILE_A = B_WT + B_WOT, TILE_BLOCKS = TILE_A + 2 * B_WG + B_WD;
__device__ __forceinline__ void tile_w_block(int c, const u16* __restrict__ Wq, const u16* __restrict__ Wk, const u16* __restrict__ Wv,
                                             const u16* __restrict__ Wo, const u16* __restrict__ Wg, const u16* __restrict__ Wu,
                                             const u16* __restrict__ Wd, char* __restrict__ ws, bool wt) {
    const u16* src; int pitch; u16* d0; int dstep;
    bool perm;
    if (c < B_WT) {
        const int nb = c & 511, kb = (c >> 9) & 63, m = c >> 15;
        const int h = nb >> 4, ks = (nb >> 2) & 3, gp = nb & 3, kk = kb >> 2, g = kb & 3;
        src = (m == 0 ? Wq : (m == 1 ? Wk : Wv)) + (size_t)(8 * kb) * 4096 + 8 * nb; pitch = 4096;
        d0 = (u16*)(ws + OFF_WT) + ((size_t)((h * 4 + ks) * 96 + m * 32 + kk) * 64 + g * 16 + 4 * gp) * 8; dstep = 16 * 64 * 8; perm = true;
    } else if ((c -= B_WT) < B_WOT) {
        const int nb = c & 63, kb = c >> 6, h = kb >> 4, ks = (kb >> 2) & 3, g = kb & 3, T = nb >> 1;
        src = Wo + (size_t)(8 * kb) * 512 + 8 * nb; pitch = 512;
        d0 = (u16*)(ws + OFF_WOT) + ((size_t)((h * 32 + T) * 4 + ks) * 64 + g * 16 + 8 * (nb & 1)) * 8; dstep = 0; perm = false;
    } else if ((c -= B_WOT) < 2 * B_WG) {
        const int which = c >= B_WG; if (which) c -= B_WG;
        const int nb = c % 224, kb = c / 224, fp = nb >> 2, gp = nb & 3, kk = kb >> 2, g = kb & 3;
        src = (which ? Wu : Wg) + (size_t)(8 * kb) * FF + 8 * nb; pitch = FF;
        d0 = (u16*)(ws + (which ? OFF_WU : OFF_WG)) + ((size_t)((2 * fp) * 16 + kk) * 64 + g * 16 + 4 * gp) * 8; dstep = 16 * 64 * 8; perm = true;
    } else {
        c -= 2 * B_WG;
        const int nb = c & 63, kb = c >> 6, kk = kb >> 2, g = kb & 3, nt = nb >> 1;
        src = Wd + (size_t)(8 * kb) * 512 + 8 * nb; pitch = 512;
        d0 = (u16*)(ws + OFF_WD) + ((size_t)(nt * 56 + kk) * 64 + g * 16 + 8 * (nb & 1)) * 8; dstep = 0; perm = false;
    }
    v4u r[8], o[8];
#pragma unroll
    for (int j = 0; j < 8; ++j) r[j] = *(const v4u*)(src + (size_t)j * pitch);
    transpose8x8(r, o);
    if (wt) {
        const __amdgpu_buffer_rsrc_t wrs = __builtin_amdgcn_make_buffer_rsrc(ws, 0, (unsigned)WS_TOTAL, 0x00020000);
        const unsigned b0 = (unsigned)((char*)d0 - ws);
#pragma unroll
        for (int j = 0; j < 8; ++j) __builtin_amdgcn_raw_buffer_store_b128(o[j], wrs, b0 + (unsigned)(perm ? (j >> 2) * dstep + (j & 3) * 8 : j * 8) * 2u, 0, 16);
    } else {
#pragma unroll
        for (int j = 0; j < 8; ++j) *(v4u*)(perm ? d0 + (j >> 2) * dstep + (j & 3) * 8 : d0 + j * 8) = o[j];
    }
}

constexpr int PREP_LN_BLOCKS = S_ / 4, PREP_TILE_BLOCKS = 65536 / 256, PREP_ZERO_BLOCKS = 256;
constexpr int ZSTEPS = 12;
constexpr size_t ZERO_A = OFF_P + (size_t)ZSTEPS * 32 * 16384;
__global__ __launch_bounds__(256) void k_prep(const u16* __restrict__ x, const u16* __restrict__ Wq, const u16* __restrict__ Wk,
                                              const u16* __restrict__ Wv, const u16* __restrict__ Wo, const u16* __restrict__ Wg,
                                              const u16* __restrict__ Wu, const u16* __restrict__ Wd, char* __restrict__ ws) {
    if (blockIdx.x < PREP_LN_BLOCKS) { ln_row(x, (u16*)(ws + OFF_XN), blockIdx.x * 4 + (threadIdx.x >> 6), threadIdx.x & 63); return; }
    if (blockIdx.x < PREP_LN_BLOCKS + PREP_TILE_BLOCKS) {
        const int c = (blockIdx.x - PREP_LN_BLOCKS) * 256 + threadIdx.x;
        tile_w_block(32768 + c, Wq, Wk, Wv, Wo, Wg, Wu, Wd, ws, false);
        return;
    }
    v4u* z = (v4u*)ws;
    for (size_t e = (size_t)(blockIdx.x - PREP_LN_BLOCKS - PREP_TILE_BLOCKS) * 256 + threadIdx.x; e < ZERO_A / 16; e += (size_t)PREP_ZERO_BLOCKS * 256)
        z[e] = (v4u){0u, 0u, 0u, 0u};
}

#define GLDS16(gp, lp) __builtin_amdgcn_global_load_lds((const unsigned*)(gp), (unsigned*)(lp), 16, 0, 0)
constexpr unsigned QKV_MAT_BYTES = (unsigned)S_ * NH * DH * 2;
__device__ __forceinline__ void qkv_tile(char* lds, const bf16x8* __restrict__ XN, const bf16x8* __restrict__ WT, __amdgpu_buffer_rsrc_t qrs,
                                         int mtile, int m, int h, int w, int lane) {
    bf16x8 (*sm)[32][64] = (bf16x8 (*)[32][64])lds;
    const int wm = w >> 1, wn = w & 1, g = lane >> 4, i = lane & 15;
    const bf16x8* sp = w < 2 ? XN + ((size_t)(mtile * 8 + w * 4) * 16) * 64 + lane
                             : WT + ((size_t)(h * 4 + (w - 2) * 2) * 96 + m * 32) * 64 + lane;
    auto stage = [&](int c, int buf) {
#pragma unroll
        for (int f = 0; f < 8; ++f) {
            const int t = f >> 1, kk = 2 * c + (f & 1);
            const int off = w < 2 ? (t * 16 + kk) * 64 : ((t >> 1) * 96 + (t & 1) * 16 + kk) * 64;
            GLDS16(sp + off, &sm[buf][w * 8 + f][0]);
        }
    };
    f32x4 acc[4][4];
#pragma unroll
    for (int a = 0; a < 4; ++a)
#pragma unroll
        for (int b = 0; b < 4; ++b) acc[a][b] = (f32x4){0.f, 0.f, 0.f, 0.f};
    __syncthreads();
    stage(0, 0);
    for (int c = 0; c < 8; ++c) {
        asm volatile("s_waitcnt vmcnt(0)" ::: "memory");
        __syncthreads();
        if (c + 1 < 8) stage(c + 1, (c + 1) & 1);
#pragma unroll
        for (int k2 = 0; k2 < 2; ++k2) {
            bf16x8 a[4], b[4];
#pragma unroll
            for (int t = 0; t < 4; ++t) { a[t] = sm[c & 1][(wm * 4 + t) * 2 + k2][lane]; b[t] = sm[c & 1][16 + (wn * 4 + t) * 2 + k2][lane]; }
            if (m < 2) {
#pragma unroll
                for (int mt = 0; mt < 4; ++mt)
#pragma unroll
                    for (int t = 0; t < 4; ++t) acc[mt][t] = MFMA(b[t], a[mt], acc[mt][t]);
            } else {
#pragma unroll
                for (int mt = 0; mt < 4; ++mt)
#pragma unroll
                    for (int t = 0; t < 4; ++t) acc[mt][t] = MFMA(a[mt], b[t], acc[mt][t]);
            }
        }
    }
    const int mb = mtile * 2 + wm;
    if (m < 2) {
#pragma unroll
        for (int mt = 0; mt < 4; ++mt) {
            const int tok = mb * 64 + mt * 16 + i, s = tok >> 5, tl = tok & 31;
            const int tile = m == 0 ? (tl >> 4) : ((tl >> 2) & 1), row = m == 0 ? (tl & 15) : (4 * (tl >> 3) + (tl & 3));
#pragma unroll
            for (int p = 0; p < 2; ++p) {
                const int ks = 2 * wn + p;
                v4u o = {pack2(acc[mt][2 * p][0], acc[mt][2 * p][1]), pack2(acc[mt][2 * p][2], acc[mt][2 * p][3]),
                         pack2(acc[mt][2 * p + 1][0], acc[mt][2 * p + 1][1]), pack2(acc[mt][2 * p + 1][2], acc[mt][2 * p + 1][3])};
                __builtin_amdgcn_raw_buffer_store_b128(o, qrs, (unsigned)m * QKV_MAT_BYTES + (unsigned)((s * NH + h) * 8192 + ((tile * 4 + ks) * 64 + g * 16 + row) * 16), 0, 16);
            }
        }
    } else {
#pragma unroll
        for (int mt = 0; mt < 4; ++mt) {
            const int s = mb * 2 + (mt >> 1), gp = 2 * (mt & 1) + (g >> 1);
#pragma unroll
            for (int t = 0; t < 4; ++t) {
                const int tau = 4 * wn + t;
                v2u o = {pack2(acc[mt][t][0], acc[mt][t][1]), pack2(acc[mt][t][2], acc[mt][t][3])};
                __builtin_amdgcn_raw_buffer_store_b64(o, qrs, 2u * QKV_MAT_BYTES + (unsigned)((s * NH + h) * 8192 + (tau * 64 + gp * 16 + i) * 16 + (g & 1) * 8), 0, 16);
            }
        }
    }
}

__device__ __forceinline__ void glu_block(const bf16x8* __restrict__ XN, const bf16x8* __restrict__ WG, const bf16x8* __restrict__ WU,
                                          const bf16x8* __restrict__ WD, u16* __restrict__ Bout, char* lds, int mb, int w, int lane) {
    const int g = lane >> 4, i = lane & 15;
    bf16x8* Hl = (bf16x8*)lds;
    const bf16x8* xa = XN + (size_t)(mb * 2) * 16 * 64 + lane;
    for (int fp = w; fp < 56; fp += 4) {
        f32x4 ag[2][2], au[2][2];
#pragma unroll
        for (int a = 0; a < 2; ++a)
#pragma unroll
            for (int b = 0; b < 2; ++b) { ag[a][b] = (f32x4){0.f, 0.f, 0.f, 0.f}; au[a][b] = (f32x4){0.f, 0.f, 0.f, 0.f}; }
        const bf16x8* wg = WG + (size_t)(fp * 2) * 16 * 64 + lane;
        const bf16x8* wu = WU + (size_t)(fp * 2) * 16 * 64 + lane;
#pragma unroll 4
        for (int kk = 0; kk < 16; ++kk) {
            bf16x8 a[2], bg[2], bu[2];
#pragma unroll
            for (int t = 0; t < 2; ++t) { a[t] = xa[(t * 16 + kk) * 64]; bg[t] = wg[(t * 16 + kk) * 64]; bu[t] = wu[(t * 16 + kk) * 64]; }
#pragma unroll
            for (int mt = 0; mt < 2; ++mt)
#pragma unroll
                for (int t = 0; t < 2; ++t) { ag[mt][t] = MFMA(bg[t], a[mt], ag[mt][t]); au[mt][t] = MFMA(bu[t], a[mt], au[mt][t]); }
        }
#pragma unroll
        for (int mt = 0; mt < 2; ++mt) {
            float hv[8];
#pragma unroll
            for (int t = 0; t < 2; ++t)
#pragma unroll
                for (int r = 0; r < 4; ++r) {
                    const float gb = rbf(ag[mt][t][r]), ub = rbf(au[mt][t][r]);
                    const float sg = rbf(1.0f / (1.0f + __expf(-gb)));
                    hv[t * 4 + r] = sg * ub;
                }
            Hl[(mt * 56 + fp) * 64 + lane] = mk8(pack2(hv[0], hv[1]), pack2(hv[2], hv[3]), pack2(hv[4], hv[5]), pack2(hv[6], hv[7]));
        }
    }
    __syncthreads();
    for (int nq = w; nq < 8; nq += 4) {
        f32x4 ab[2][4];
#pragma unroll
        for (int a = 0; a < 2; ++a)
#pragma unroll
            for (int b = 0; b < 4; ++b) ab[a][b] = (f32x4){0.f, 0.f, 0.f, 0.f};
        const bf16x8* pb = WD + (size_t)(nq * 4) * 56 * 64 + lane;
#pragma unroll 4
        for (int kk = 0; kk < 56; ++kk) {
            const bf16x8 a0 = Hl[kk * 64 + lane], a1 = Hl[(56 + kk) * 64 + lane];
#pragma unroll
            for (int t = 0; t < 4; ++t) {
                const bf16x8 b = pb[(t * 56 + kk) * 64];
                ab[0][t] = MFMA(b, a0, ab[0][t]); ab[1][t] = MFMA(b, a1, ab[1][t]);
            }
        }
#pragma unroll
        for (int mt = 0; mt < 2; ++mt)
#pragma unroll
            for (int t = 0; t < 4; ++t)
                *(v2u*)(Bout + (size_t)(mb * 32 + mt * 16 + i) * DM + (nq * 4 + t) * 16 + 4 * g) =
                    (v2u){pack2(ab[mt][t][0], ab[mt][t][1]), pack2(ab[mt][t][2], ab[mt][t][3])};
    }
}

constexpr int L_WO = 0, L_MEM = 131072, L_SC = L_MEM + 8192, L_OE = L_SC + 6144, L_PX = L_OE + 2048, L_TOTAL = L_PX + 4 * 640;
constexpr unsigned SPINCAP = 1u << 20;
#define JS 4
constexpr int NCH = NH * JS;
constexpr int NWORK = 256 - NCH;
constexpr int TPW = 8 / JS;
constexpr float SCALE = 0.08838834764831845f;
#define SLP1 10
#define AUXS 16
#define AUXL 16
#define SLPN 16
#define SLP2 15

__device__ __forceinline__ float xor32_max(float x) { auto r = __builtin_amdgcn_permlane32_swap(__float_as_uint(x), __float_as_uint(x), false, false); return fmaxf(__uint_as_float(r[0]), __uint_as_float(r[1])); }
__device__ __forceinline__ float xor16_max(float x) { auto r = __builtin_amdgcn_permlane16_swap(__float_as_uint(x), __float_as_uint(x), false, false); return fmaxf(__uint_as_float(r[0]), __uint_as_float(r[1])); }
__device__ __forceinline__ float xor32_add(float x) { auto r = __builtin_amdgcn_permlane32_swap(__float_as_uint(x), __float_as_uint(x), false, false); return __uint_as_float(r[0]) + __uint_as_float(r[1]); }
__device__ __forceinline__ float xor16_add(float x) { auto r = __builtin_amdgcn_permlane16_swap(__float_as_uint(x), __float_as_uint(x), false, false); return __uint_as_float(r[0]) + __uint_as_float(r[1]); }
template <int CTRL> __device__ __forceinline__ float dpp_f(float x) { return __int_as_float(__builtin_amdgcn_update_dpp(0, __float_as_int(x), CTRL, 0xf, 0xf, false)); }
__device__ __forceinline__ float ror8(float x) { return __int_as_float(__builtin_amdgcn_update_dpp(0, __float_as_int(x), 0x128, 0xf, 0xf, false)); }

typedef __attribute__((ext_vector_type(2))) float f32x2;
__device__ __forceinline__ unsigned cvtpk(float a, float b) { unsigned r; asm("v_cvt_pk_bf16_f32 %0, %1, %2" : "=v"(r) : "v"(a), "v"(b)); return r; }
__device__ __forceinline__ float vmax(float a, float b) { float r; asm("v_max_f32 %0, %1, %2" : "=v"(r) : "v"(a), "v"(b)); return r; }
__device__ __forceinline__ float vmax3(float a, float b, float c) { float r; asm("v_max3_f32 %0, %1, %2, %3" : "=v"(r) : "v"(a), "v"(b), "v"(c)); return r; }
__device__ __forceinline__ float xmax32(float x) { auto r = __builtin_amdgcn_permlane32_swap(__float_as_uint(x), __float_as_uint(x), false, false); return vmax(__uint_as_float(r[0]), __uint_as_float(r[1])); }
__device__ __forceinline__ float xmax16(float x) { auto r = __builtin_amdgcn_permlane16_swap(__float_as_uint(x), __float_as_uint(x), false, false); return vmax(__uint_as_float(r[0]), __uint_as_float(r[1])); }
constexpr unsigned KM_BYTES = (unsigned)NSEG * NH * MT * DH * 2;
__device__ __forceinline__ void xattn_unit(__amdgpu_buffer_rsrc_t qrs, __amdgpu_buffer_rsrc_t krs, bf16x8* dst, int s, int h, int lane) {
    const int g = lane >> 4, i = lane & 15;
    const unsigned qo = (unsigned)((s * NH + h) * 8192 + lane * 16), mo = (unsigned)((s * NH + h) * 2048);
#define LDQ(off) __builtin_bit_cast(bf16x8, __builtin_amdgcn_raw_buffer_load_b128(qrs, (off), 0, 0))
    bf16x8 kf[4], k1[4], k2[4], qf[2][4], vx[8]; v2u vm[8];
#pragma unroll
    for (int ks = 0; ks < 4; ++ks) {
        kf[ks] = __builtin_bit_cast(bf16x8, __builtin_amdgcn_raw_buffer_load_b128(krs, mo + (unsigned)((i & 7) * 256 + 64 * ks + 16 * g), 0, 0));
        k1[ks] = LDQ(QKV_MAT_BYTES + qo + ks * 1024);
        k2[ks] = LDQ(QKV_MAT_BYTES + qo + (4 + ks) * 1024);
        qf[0][ks] = LDQ(qo + ks * 1024);
        qf[1][ks] = LDQ(qo + (4 + ks) * 1024);
    }
#pragma unroll
    for (int tau = 0; tau < 8; ++tau) {
        vx[tau] = LDQ(2u * QKV_MAT_BYTES + qo + tau * 1024);
        vm[tau] = __builtin_amdgcn_raw_buffer_load_b64(krs, KM_BYTES + mo + (unsigned)((tau * 32 + (lane & 31)) * 8), 0, 0);
    }
#pragma unroll
    for (int qt = 0; qt < 2; ++qt) {
        f32x4 sc[3];
#pragma unroll
        for (int a = 0; a < 3; ++a) sc[a] = (f32x4){0.f, 0.f, 0.f, 0.f};
#pragma unroll
        for (int ks = 0; ks < 4; ++ks) {
            sc[0] = MFMA(kf[ks], qf[qt][ks], sc[0]);
            sc[1] = MFMA(k1[ks], qf[qt][ks], sc[1]);
            sc[2] = MFMA(k2[ks], qf[qt][ks], sc[2]);
        }
        const int tq = qt * 16 + i;
        float sv[12]; bool ok[12];
#pragma unroll
        for (int r = 0; r < 4; ++r) { ok[r] = g < 2; ok[4 + r] = (8 * g + r) <= tq; ok[8 + r] = (8 * g + 4 + r) <= tq; }
        float mx = -3.0e38f;
#pragma unroll
        for (int a = 0; a < 3; ++a)
#pragma unroll
            for (int r = 0; r < 4; ++r) { sv[a * 4 + r] = rbf(rbf(sc[a][r]) * SCALE); mx = ok[a * 4 + r] ? fmaxf(mx, sv[a * 4 + r]) : mx; }
        mx = xor16_max(mx);
        mx = xor32_max(mx);
        float ex[12], sum = 0.f;
#pragma unroll
        for (int a = 0; a < 12; ++a) { ex[a] = ok[a] ? __expf(sv[a] - mx) : 0.f; sum += ex[a]; }
        sum = xor16_add(sum);
        sum = xor32_add(sum);
        const float inv = 1.0f / sum;
        const bf16x8 pX = mk8(pack2(ex[4] * inv, ex[5] * inv), pack2(ex[6] * inv, ex[7] * inv), pack2(ex[8] * inv, ex[9] * inv), pack2(ex[10] * inv, ex[11] * inv));
        const bf16x8 pM = mk8(pack2(ex[0] * inv, ex[1] * inv), pack2(ex[2] * inv, ex[3] * inv), 0u, 0u);
#pragma unroll
        for (int ks = 0; ks < 4; ++ks) {
            f32x4 o[2];
#pragma unroll
            for (int t = 0; t < 2; ++t) {
                const int tau = 2 * ks + t;
                o[t] = MFMA(vx[tau], pX, ((f32x4){0.f, 0.f, 0.f, 0.f}));
                o[t] = MFMA(mk8(vm[tau].x, vm[tau].y, 0u, 0u), pM, o[t]);
            }
            dst[(qt * 16 + ks) * 64 + lane] =
                mk8(pack2(o[0][0], o[0][1]), pack2(o[0][2], o[0][3]), pack2(o[1][0], o[1][1]), pack2(o[1][2], o[1][3]));
        }
    }
}

#define AMFMA0(d, a, b) asm volatile("s_nop 1\n\tv_mfma_f32_16x16x32_bf16 %0, %1, %2, 0" : "=&v"(d) : "v"(a), "v"(b))
#define AMFMA(d, a, b) asm volatile("s_nop 1\n\tv_mfma_f32_16x16x32_bf16 %0, %1, %2, %0" : "+v"(d) : "v"(a), "v"(b))
#define AMFMA16(d, a, b) asm volatile("s_nop 1\n\tv_mfma_f32_16x16x16_bf16 %0, %1, %2, %0" : "+v"(d) : "v"(a), "v"(b))

__global__ __launch_bounds__(256, 1) void k_chain(const u16* __restrict__ mem0, const bf16x8* __restrict__ WT, const bf16x8* __restrict__ WOT,
                                                  u16* __restrict__ KM, u16* __restrict__ VM, unsigned* Pbuf, unsigned* Mbuf, unsigned* tmo,
                                                  const bf16x8* __restrict__ XN, const bf16x8* __restrict__ WG, const bf16x8* __restrict__ WU,
                                                  const bf16x8* __restrict__ WD, u16* __restrict__ QX, u16* __restrict__ Bout,
                                                  const u16* __restrict__ Wg, const u16* __restrict__ Wu, const u16* __restrict__ Wd, char* ws,
                                                  const u16* __restrict__ Wq, const u16* __restrict__ Wk, const u16* __restrict__ Wv,
                                                  const u16* __restrict__ Wo) {
    extern __shared__ __attribute__((aligned(16))) char lds[];
    const int h = blockIdx.x & 31, jj = blockIdx.x >> 5;
    const int w = __builtin_amdgcn_readfirstlane(threadIdx.x >> 6), lane = threadIdx.x & 63, g = lane >> 4, i = lane & 15;
    const __amdgpu_buffer_rsrc_t qrs = __builtin_amdgcn_make_buffer_rsrc(QX, 0, 3u * QKV_MAT_BYTES, 0x00020000);
    const __amdgpu_buffer_rsrc_t krs = __builtin_amdgcn_make_buffer_rsrc(KM, 0, 2u * KM_BYTES, 0x00020000);
    unsigned* kvcnt = tmo + 16;
    unsigned* zcnt = tmo + 64;
    if (blockIdx.x >= NCH) {
        const int wb = blockIdx.x - NCH;
        for (int t = wb; t < 1024; t += NWORK) {
            qkv_tile(lds, XN, WT, qrs, t >> 6, 1 + ((t >> 5) & 1), t & 31, w, lane);
            asm volatile("s_waitcnt vmcnt(0)" ::: "memory");
            __syncthreads();
            if (threadIdx.x == 0) __hip_atomic_fetch_add(kvcnt + (t >> 6), 1u, __ATOMIC_RELAXED, __HIP_MEMORY_SCOPE_AGENT);
            if (t == wb)
            {
                const __amdgpu_buffer_rsrc_t zrs = __builtin_amdgcn_make_buffer_rsrc(Pbuf, 0, 63 * 32 * 16384, 0x00020000);
                for (unsigned e = (unsigned)ZSTEPS * 32768u + (unsigned)(wb * 256 + threadIdx.x); e < 63u * 32768u; e += (unsigned)NWORK * 256u)
                    __builtin_amdgcn_raw_buffer_store_b128((v4u){0u, 0u, 0u, 0u}, zrs, e * 16u, 0, 16);
                static_assert(NWORK == 128, "work split");
                tile_w_block(wb * 256 + threadIdx.x, Wq, Wk, Wv, Wo, Wg, Wu, Wd, ws, true);
#pragma unroll 2
                for (int c = B_WT + wb * 256 + threadIdx.x; c < TILE_BLOCKS; c += NWORK * 256) tile_w_block(c, Wq, Wk, Wv, Wo, Wg, Wu, Wd, ws, true);
                asm volatile("s_waitcnt vmcnt(0)" ::: "memory");
                __syncthreads();
                if (threadIdx.x == 0) __hip_atomic_fetch_add(zcnt, 1u, __ATOMIC_RELAXED, __HIP_MEMORY_SCOPE_AGENT);
            }
        }
        __syncthreads();
        {
            if (threadIdx.x == 0) {
                unsigned spins = 0;
                while (__hip_atomic_load(zcnt, __ATOMIC_RELAXED, __HIP_MEMORY_SCOPE_AGENT) < (unsigned)NWORK) {
                    __builtin_amdgcn_s_sleep(8);
                    if (++spins > SPINCAP) { atomicAdd(tmo, 1u); break; }
                }
                __builtin_amdgcn_fence(__ATOMIC_ACQUIRE, "agent");
                asm volatile("s_waitcnt vmcnt(0)" ::: "memory");
            }
            __syncthreads();
        }
        if (wb < 64) {
            glu_block(XN, WG, WU, WD, Bout, lds, wb, w, lane);
        } else {
            for (int t = wb - 64; t < 512; t += NWORK - 64) {
                qkv_tile(lds, XN, WT, qrs, t >> 5, 0, t & 31, w, lane);
            }
        }
        return;
    }
    unsigned kv_ready = 0;
    auto wait_kv = [&](int grp) {
        unsigned spins = 0;
        while (!((kv_ready >> grp) & 1u)) {
            const unsigned c = __hip_atomic_load(kvcnt + (lane & 15), __ATOMIC_RELAXED, __HIP_MEMORY_SCOPE_AGENT);
            kv_ready = (unsigned)__ballot(c >= 64u) & 0xffffu;
            if ((kv_ready >> grp) & 1u) break;
            __builtin_amdgcn_s_sleep(2);
            if (++spins > SPINCAP) { if (lane == 0) atomicAdd(tmo, 1u); break; }
        }
    };
    auto fetch_kv = [&](int sg, v4u (&pk)[2], v4u (&pv)[2]) {
        const unsigned o = (unsigned)((sg * NH + h) * 8192 + lane * 16);
        pk[0] = __builtin_amdgcn_raw_buffer_load_b128(qrs, QKV_MAT_BYTES + o + w * 1024, 0, 16);
        pk[1] = __builtin_amdgcn_raw_buffer_load_b128(qrs, QKV_MAT_BYTES + o + w * 1024 + 4096, 0, 16);
        pv[0] = __builtin_amdgcn_raw_buffer_load_b128(qrs, 2u * QKV_MAT_BYTES + o + w * 2048, 0, 16);
        pv[1] = __builtin_amdgcn_raw_buffer_load_b128(qrs, 2u * QKV_MAT_BYTES + o + w * 2048 + 1024, 0, 16);
    };
    const __amdgpu_buffer_rsrc_t prs = __builtin_amdgcn_make_buffer_rsrc(Pbuf, 0, 63 * 32 * 16384, 0x00020000);
    const __amdgpu_buffer_rsrc_t mrs = __builtin_amdgcn_make_buffer_rsrc(Mbuf, 0, 64 * 8192, 0x00020000);

    static_assert(JS == 4, "the Wo self-load below assumes 2 output tiles per wave");
    bf16x8 WA[64], WV[32];
    {
        char* stg = lds + 32768 + w * 16384;
        const int cb = lane & 3;
#pragma unroll
        for (int ps = 0; ps < 6; ++ps) {
            const int m = ps >> 1, half = ps & 1;
            const u16* Wm = m == 0 ? Wq : (m == 1 ? Wk : Wv);
#pragma unroll
            for (int it = 0; it < 2; ++it) {
                const int kbl = (lane >> 2) + 16 * it;
                const u16* sp = Wm + (size_t)(8 * (half * 32 + kbl)) * 4096 + h * DH + 32 * w + 8 * cb;
                v4u r[8], o[8];
#pragma unroll
                for (int j = 0; j < 8; ++j) r[j] = *(const v4u*)(sp + (size_t)j * 4096);
                transpose8x8(r, o);
                char* d = stg + (kbl >> 2) * 1024 + ((kbl & 3) * 16 + 4 * cb) * 16;
#pragma unroll
                for (int j = 0; j < 8; ++j) *(v4u*)(d + (j >> 2) * 8192 + (j & 3) * 16) = o[j];
            }
            __builtin_amdgcn_wave_barrier();
#pragma unroll
            for (int f = 0; f < 16; ++f) {
                const bf16x8 v = *(const bf16x8*)(stg + f * 1024 + lane * 16);
                const int idx = (f >> 3) * 16 + half * 8 + (f & 7);
                if (m < 2) WA[m * 32 + idx] = v; else WV[idx] = v;
            }
            asm volatile("s_waitcnt lgkmcnt(0)" ::: "memory");
            __builtin_amdgcn_wave_barrier();
        }
        {
            const int kb = lane >> 2;
            const u16* sp = Wo + (size_t)(h * DH + 8 * kb) * DM + 32 * (jj * 4 + w) + 8 * cb;
            v4u r[8], o[8];
#pragma unroll
            for (int j = 0; j < 8; ++j) r[j] = *(const v4u*)(sp + (size_t)j * DM);
            transpose8x8(r, o);
            char* d = lds + L_WO + (w * 8 + (cb >> 1) * 4 + (kb >> 2)) * 1024 + ((kb & 3) * 16 + 8 * (cb & 1)) * 16;
#pragma unroll
            for (int j = 0; j < 8; ++j) *(v4u*)(d + j * 16) = o[j];
        }
    }
    {
        const int tid = threadIdx.x;
#pragma unroll
        for (int c = tid; c < 512; c += 256) {
            const int t = c & 7, gg = (c >> 3) & 3, kk = c >> 5;
            *(v4u*)(lds + L_MEM + c * 16) = *(const v4u*)(mem0 + t * DM + 32 * kk + 8 * gg);
        }
    }
    v4u pk[2], pv[2];
    wait_kv(0);
    fetch_kv(0, pk, pv);
    const int rid = blockIdx.x * 4 + w;
    const bool is_red = (rid % JS) == 0;
    const int red = rid / JS;
#define STAMP(k)
    for (int s = 0; s < NSEG; ++s) {
        __syncthreads();
        STAMP(0);
        asm volatile("" : "+v"(pk[0]), "+v"(pk[1]), "+v"(pv[0]), "+v"(pv[1]));
        f32x4 acc[6];
        bf16x8 mf[16];
        const char* mp = lds + L_MEM + g * 128 + (i & 7) * 16;
        {
#pragma unroll
            for (int kk = 0; kk < 4; ++kk) mf[kk] = *(const bf16x8*)(mp + kk * 512);
#pragma unroll
            for (int kk = 0; kk < 16; ++kk) {
                if (kk + 4 < 16) mf[kk + 4] = *(const bf16x8*)(mp + (kk + 4) * 512);
                if (kk == 0) {
                    asm volatile("v_mfma_f32_16x16x32_bf16 %0, %4, %8, 0\n\tv_mfma_f32_16x16x32_bf16 %1, %5, %8, 0\n\t"
                                 "v_mfma_f32_16x16x32_bf16 %2, %6, %8, 0\n\tv_mfma_f32_16x16x32_bf16 %3, %7, %8, 0"
                                 : "=&v"(acc[0]), "=&v"(acc[1]), "=&v"(acc[2]), "=&v"(acc[3])
                                 : "a"(WA[0]), "a"(WA[16]), "a"(WA[32]), "a"(WA[48]), "v"(mf[0]));
                } else {
                    asm volatile("v_mfma_f32_16x16x32_bf16 %0, %4, %8, %0\n\tv_mfma_f32_16x16x32_bf16 %1, %5, %8, %1\n\t"
                                 "v_mfma_f32_16x16x32_bf16 %2, %6, %8, %2\n\tv_mfma_f32_16x16x32_bf16 %3, %7, %8, %3"
                                 : "+v"(acc[0]), "+v"(acc[1]), "+v"(acc[2]), "+v"(acc[3])
                                 : "a"(WA[kk]), "a"(WA[16 + kk]), "a"(WA[32 + kk]), "a"(WA[48 + kk]), "v"(mf[kk]));
                }
            }
            asm volatile("s_nop 7\n\ts_nop 1" : "+v"(acc[0]), "+v"(acc[1]), "+v"(acc[2]), "+v"(acc[3]));
        }
        const bf16x8 qf = mk8(pack2(acc[0][0], acc[0][1]), pack2(acc[0][2], acc[0][3]), pack2(acc[1][0], acc[1][1]), pack2(acc[1][2], acc[1][3]));
        const bf16x8 kf = mk8(pack2(acc[2][0], acc[2][1]), pack2(acc[2][2], acc[2][3]), pack2(acc[3][0], acc[3][1]), pack2(acc[3][2], acc[3][3]));
        if (i < 8 && jj == 0) __builtin_amdgcn_raw_buffer_store_b128(__builtin_bit_cast(v4u, kf), krs, (unsigned)((s * NH + h) * 2048 + i * 256 + 64 * w + 16 * g), 0, 0);
        {
            f32x4 sp[3];
            AMFMA0(sp[0], kf, qf); AMFMA0(sp[1], pk[0], qf); AMFMA0(sp[2], pk[1], qf);
            asm volatile("s_nop 7" : "+v"(sp[0]), "+v"(sp[1]), "+v"(sp[2]));
            if (i < 8) {
#pragma unroll
                for (int a = 0; a < 3; ++a) *(f32x4*)(lds + L_SC + (((w * 3 + a) * 4 + g) * 8 + i) * 16) = sp[a];
            }
        }
#pragma unroll
        for (int kk = 0; kk < 4; ++kk) mf[kk] = *(const bf16x8*)(mp + kk * 512);
        STAMP(1);
        __syncthreads();
        STAMP(2);
#define VS(kk, hf)                                                                                                             \
        do {                                                                                                                   \
            if ((hf) == 0 && (kk) + 4 < 16) mf[((kk) + 4) & 15] = *(const bf16x8*)(mp + ((kk) + 4) * 512);                     \
            if ((kk) == 0) asm volatile("v_mfma_f32_16x16x32_bf16 %0, %2, %1, 0" : "=&v"(acc[4 + (hf)]) : "v"(WV[16 * (hf)]), "v"(mf[0]));      \
            else asm volatile("v_mfma_f32_16x16x32_bf16 %0, %2, %1, %0" : "+v"(acc[4 + (hf)]) : "v"(WV[16 * (hf) + (kk)]), "v"(mf[kk]));        \
        } while (0)
        v2u v0, v1;
        bf16x8 wf[4][8];
        const char* wp = lds + L_WO + (w * TPW * 4) * 1024 + lane * 16;
        {
            const int q = lane & 7, kg = lane >> 3;
            const char* sx = lds + L_SC + (((1 + (kg & 1)) * 4 + (kg >> 1)) * 8 + q) * 16;
            const char* sm_ = lds + L_SC + ((kg >> 2) * 8 + q) * 16 + (kg & 3) * 4;
            f32x4 px[4]; float pm[4];
#pragma unroll
            for (int ww = 0; ww < 4; ++ww) { px[ww] = *(const f32x4*)(sx + ww * 1536); pm[ww] = *(const float*)(sm_ + ww * 1536); }
            VS(0, 0); VS(0, 1); VS(1, 0); VS(1, 1); VS(2, 0); VS(2, 1); VS(3, 0); VS(3, 1);
            const f32x4 tx = (px[0] + px[1]) + (px[2] + px[3]);
            VS(4, 0);
            const float tm = (pm[0] + pm[1]) + (pm[2] + pm[3]);
            VS(4, 1);
            const unsigned u0 = cvtpk(tx[0], tx[1]), u1 = cvtpk(tx[2], tx[3]), u2 = cvtpk(tm, tm);
            VS(5, 0);
            const f32x4 bx = (f32x4){__uint_as_float(u0 << 16), __uint_as_float(u0 & 0xffff0000u), __uint_as_float(u1 << 16), __uint_as_float(u1 & 0xffff0000u)} * SCALE;
            const float bm = __uint_as_float(u2 << 16) * SCALE;
            VS(5, 1);
            const unsigned w0 = cvtpk(bx[0], bx[1]), w1 = cvtpk(bx[2], bx[3]), w2 = cvtpk(bm, bm);
            VS(6, 0);
            const float s0 = __uint_as_float(w0 << 16), s1 = __uint_as_float(w0 & 0xffff0000u), s2 = __uint_as_float(w1 << 16),
                        s3 = __uint_as_float(w1 & 0xffff0000u), s4 = __uint_as_float(w2 << 16);
            VS(6, 1);
            float mx = vmax3(vmax3(s0, s1, s2), s3, s4);
            VS(7, 0);
            mx = vmax(mx, ror8(mx)); VS(7, 1);
            mx = xmax16(mx); VS(8, 0);
            mx = xmax32(mx); VS(8, 1);
            constexpr float L2E = 1.4426950408889634f;
            const float nm = -mx * L2E;
            const float e0 = __builtin_amdgcn_exp2f(__builtin_fmaf(s0, L2E, nm)), e1 = __builtin_amdgcn_exp2f(__builtin_fmaf(s1, L2E, nm));
            VS(9, 0);
            const float e2 = __builtin_amdgcn_exp2f(__builtin_fmaf(s2, L2E, nm)), e3 = __builtin_amdgcn_exp2f(__builtin_fmaf(s3, L2E, nm));
            VS(9, 1);
            const float e4 = __builtin_amdgcn_exp2f(__builtin_fmaf(s4, L2E, nm));
            float sum = ((e0 + e1) + (e2 + e3)) + e4;
            VS(10, 0);
            sum += ror8(sum); VS(10, 1);
            sum = xor16_add(sum); VS(11, 0);
            sum = xor32_add(sum); VS(11, 1);
            const float inv = __builtin_amdgcn_rcpf(sum);
            char* pw = lds + L_PX + w * 640;
            *(v2u*)(pw + q * 64 + kg * 8) = (v2u){cvtpk(e0 * inv, e1 * inv), cvtpk(e2 * inv, e3 * inv)};
            *(u16*)(pw + 512 + q * 16 + kg * 2) = (u16)cvtpk(e4 * inv, e4 * inv);
            VS(12, 0); VS(12, 1); VS(13, 0); VS(13, 1);
            __builtin_amdgcn_wave_barrier();
            const bf16x8 pX = *(const bf16x8*)(pw + (i & 7) * 64 + g * 16);
            v2u pM = *(const v2u*)(pw + 512 + (i & 7) * 16 + (g & 1) * 8);
#pragma unroll
            for (int f = 0; f < 8; ++f) wf[0][f] = *(const bf16x8*)(wp + f * 1024);
            VS(14, 0); VS(14, 1); VS(15, 0); VS(15, 1);
            asm volatile("s_nop 7" : "+v"(acc[4]), "+v"(acc[5]));
            v0 = (v2u){cvtpk(acc[4][0], acc[4][1]), cvtpk(acc[4][2], acc[4][3])};
            v1 = (v2u){cvtpk(acc[5][0], acc[5][1]), cvtpk(acc[5][2], acc[5][3])};
            if (g >= 2) pM = (v2u){0u, 0u};
            f32x4 o0, o1;
            AMFMA0(o0, pv[0], pX); AMFMA0(o1, pv[1], pX);
            asm volatile("s_nop 5" : "+v"(o0), "+v"(o1));
            AMFMA16(o0, v0, pM); AMFMA16(o1, v1, pM);
            asm volatile("s_nop 7" : "+v"(o0), "+v"(o1));
            if (i < 8) *(bf16x8*)(lds + L_OE + ((w * 4 + g) * 8 + i) * 16) = mk8(cvtpk(o0[0], o0[1]), cvtpk(o0[2], o0[3]), cvtpk(o1[0], o1[1]), cvtpk(o1[2], o1[3]));
        }
        if (g < 2 && jj == 0) {
            __builtin_amdgcn_raw_buffer_store_b64(v0, krs, KM_BYTES + (unsigned)((s * NH + h) * 2048 + (2 * w * 32 + lane) * 8), 0, 0);
            __builtin_amdgcn_raw_buffer_store_b64(v1, krs, KM_BYTES + (unsigned)((s * NH + h) * 2048 + ((2 * w + 1) * 32 + lane) * 8), 0, 0);
        }
        if (s == NSEG - 1) break;
        const unsigned pofs = (unsigned)s * (32u * 16384u);
        if (s == ZSTEPS) {
            unsigned spins = 0;
            while ((unsigned)__builtin_amdgcn_readfirstlane(__hip_atomic_load(zcnt, __ATOMIC_RELAXED, __HIP_MEMORY_SCOPE_AGENT)) < (unsigned)NWORK) {
                __builtin_amdgcn_s_sleep(2);
                if (++spins > SPINCAP) { if (lane == 0) atomicAdd(tmo, 1u); break; }
            }
        }
        {
            __syncthreads();
            STAMP(3);
            bf16x8 of[4];
#pragma unroll
            for (int ks = 0; ks < 4; ++ks) of[ks] = *(const bf16x8*)(lds + L_OE + ((ks * 4 + g) * 8 + (i & 7)) * 16);
#pragma unroll
            for (int a = 0; a < TPW / 2; ++a) {
                if (a + 1 < TPW / 2) {
#pragma unroll
                    for (int f = 0; f < 8; ++f) wf[a + 1][f] = *(const bf16x8*)(wp + ((a + 1) * 8 + f) * 1024);
                }
                f32x4 d0, d1;
                AMFMA0(d0, wf[a][0], of[0]); AMFMA0(d1, wf[a][4], of[0]);
#pragma unroll
                for (int ks = 1; ks < 4; ++ks) { AMFMA(d0, wf[a][ks], of[ks]); AMFMA(d1, wf[a][4 + ks], of[ks]); }
                asm volatile("s_nop 7" : "+v"(d0), "+v"(d1));
                v4u v;
#pragma unroll
                for (int r = 0; r < 4; ++r) {
                    const float x1 = ror8(d1[r]);
                    v[r] = __float_as_uint(i < 8 ? d0[r] : x1) | 1u;
                }
                const int T = (jj * 4 + w) * TPW + 2 * a + (i >> 3);
                __builtin_amdgcn_raw_buffer_store_b128(v, prs, pofs + (unsigned)(h * 16384 + (4 * T + g) * 128 + (i & 7) * 16), 0, AUXS);
            }
        }
        STAMP(4);
        if (((s + 1) & 3) == 0) wait_kv((s + 1) >> 2);
        fetch_kv(s + 1, pk, pv);
        for (int z = 0; z < SLP1; ++z) __builtin_amdgcn_s_sleep(1);
        if (!is_red) {
            for (int z = 0; z < SLPN; ++z) __builtin_amdgcn_s_sleep(1);
        } else {
            v4u r[4];
            unsigned spins = 0;
            for (;;) {
#pragma unroll
                for (int q = 0; q < 4; ++q)
                    r[q] = __builtin_amdgcn_raw_buffer_load_b128(prs, pofs + (unsigned)((q * 8 + (lane >> 3)) * 16384 + red * 128 + (lane & 7) * 16), 0, AUXL);
                bool ok = true;
#pragma unroll
                for (int q = 0; q < 4; ++q) ok &= (r[q].x & r[q].y & r[q].z & r[q].w & 1u) != 0u;
                if (__all(ok)) break;
                if (++spins > SPINCAP) { if (lane == 0) atomicAdd(tmo, 1u); break; }
            }
            STAMP(5);
            float sm[4];
#pragma unroll
            for (int c = 0; c < 4; ++c) {
                sm[c] = (__uint_as_float(r[0][c] & ~1u) + __uint_as_float(r[1][c] & ~1u)) + (__uint_as_float(r[2][c] & ~1u) + __uint_as_float(r[3][c] & ~1u));
                sm[c] += ror8(sm[c]);
                sm[c] = xor16_add(sm[c]);
                sm[c] = xor32_add(sm[c]);
            }
            if (lane < 8) {
                v2u v = {pack2(sm[0], sm[1]), pack2(sm[2], sm[3])};
                if (v.x == 0u) v.x = 0x8000u;
                if (v.y == 0u) v.y = 0x8000u;
                __builtin_amdgcn_raw_buffer_store_b64(v, mrs, (unsigned)(s * 8192 + red * 64 + lane * 8), 0, AUXS);
            }
        }
        STAMP(6);
        for (int z = 0; z < SLP2; ++z) __builtin_amdgcn_s_sleep(1);
        {
            v4u m[2];
            unsigned spins = 0;
            for (;;) {
#pragma unroll
                for (int q = 0; q < 2; ++q) m[q] = __builtin_amdgcn_raw_buffer_load_b128(mrs, (unsigned)(s * 8192 + w * 2048 + q * 1024 + lane * 16), 0, AUXL);
                bool ok = true;
#pragma unroll
                for (int q = 0; q < 2; ++q) ok &= (m[q].x != 0u) & (m[q].y != 0u) & (m[q].z != 0u) & (m[q].w != 0u);
                if (__all(ok)) break;
                if (++spins > SPINCAP) { if (lane == 0) atomicAdd(tmo, 1u); break; }
            }
#pragma unroll
            for (int q = 0; q < 2; ++q) {
                const int R = w * 32 + q * 16 + (lane >> 2), t = 2 * (lane & 3);
                char* dst = lds + L_MEM + (R >> 3) * 512 + ((R >> 1) & 3) * 128 + t * 16 + (R & 1) * 8;
                *(v2u*)dst = (v2u){m[q].x, m[q].y};
                *(v2u*)(dst + 16) = (v2u){m[q].z, m[q].w};
            }
        }
        STAMP(7);
    }
}

__global__ __launch_bounds__(512, 2) void k_post(u16* QX, u16* KM, const bf16x8* __restrict__ WOT, f32x4* __restrict__ part) {
    __shared__ bf16x8 sa[4][16][64];
    const __amdgpu_buffer_rsrc_t qrs = __builtin_amdgcn_make_buffer_rsrc(QX, 0, 3u * QKV_MAT_BYTES, 0x00020000);
    const __amdgpu_buffer_rsrc_t krs = __builtin_amdgcn_make_buffer_rsrc(KM, 0, 2u * KM_BYTES, 0x00020000);
    const int w = __builtin_amdgcn_readfirstlane(threadIdx.x >> 6), lane = threadIdx.x & 63;
    const int kq = blockIdx.x & 7, mb = blockIdx.x >> 3;
    const bf16x8* pb = WOT + ((size_t)(kq * 4) * 32 + w * 4) * 4 * 64 + lane;
    bf16x8 b[4][4];
#pragma unroll
    for (int pk = 0; pk < 3; ++pk)
#pragma unroll
        for (int t = 0; t < 4; ++t) b[pk][t] = pb[(t * 4 + pk) * 64];
    xattn_unit(qrs, krs, &sa[(w >> 2) * 2][(w & 3) * 4][0], mb * 2 + (w >> 2), kq * 4 + (w & 3), lane);
    __syncthreads();
    f32x4 acc[4][4];
#pragma unroll
    for (int a = 0; a < 4; ++a)
#pragma unroll
        for (int t = 0; t < 4; ++t) acc[a][t] = (f32x4){0.f, 0.f, 0.f, 0.f};
#pragma unroll
    for (int kk = 0; kk < 16; ++kk) {
        if (kk + 3 < 16) {
            const int k2 = kk + 3;
#pragma unroll
            for (int t = 0; t < 4; ++t) b[k2 & 3][t] = pb[(((k2 >> 2) * 32 + t) * 4 + (k2 & 3)) * 64];
        }
        __builtin_amdgcn_sched_barrier(0);
        bf16x8 a[4];
#pragma unroll
        for (int mt = 0; mt < 4; ++mt) a[mt] = sa[mt][kk][lane];
#pragma unroll
        for (int mt = 0; mt < 4; ++mt)
#pragma unroll
            for (int t = 0; t < 4; ++t) acc[mt][t] = MFMA(b[kk & 3][t], a[mt], acc[mt][t]);
        __builtin_amdgcn_sched_barrier(0);
    }
#pragma unroll
    for (int mt = 0; mt < 4; ++mt)
#pragma unroll
        for (int t = 0; t < 4; ++t) part[(((size_t)kq * 128 + mb * 4 + mt) * 32 + w * 4 + t) * 64 + lane] = acc[mt][t];
}

__global__ __launch_bounds__(256) void k_out(const f32x4* __restrict__ part, const u16* __restrict__ Bout, const u16* __restrict__ x,
                                             u16* __restrict__ out) {
    const int id = blockIdx.x * 256 + threadIdx.x, lane = id & 63, tile = id >> 6, g = lane >> 4, i = lane & 15;
    constexpr size_t KS = (size_t)128 * 32 * 64;
    const f32x4 a = ((part[id] + part[KS + id]) + (part[2 * KS + id] + part[3 * KS + id])) +
                    ((part[4 * KS + id] + part[5 * KS + id]) + (part[6 * KS + id] + part[7 * KS + id]));
    const size_t idx = (size_t)((tile >> 5) * 16 + i) * DM + (tile & 31) * 16 + 4 * g;
    const v2u xr = *(const v2u*)(x + idx), br = *(const v2u*)(Bout + idx);
    const float xv[4] = {__uint_as_float(xr.x << 16), __uint_as_float(xr.x & 0xffff0000u), __uint_as_float(xr.y << 16), __uint_as_float(xr.y & 0xffff0000u)};
    const float bv[4] = {__uint_as_float(br.x << 16), __uint_as_float(br.x & 0xffff0000u), __uint_as_float(br.y << 16), __uint_as_float(br.y & 0xffff0000u)};
    float y[4];
#pragma unroll
    for (int r = 0; r < 4; ++r) y[r] = rbf(xv[r] + rbf(a[r])) + bv[r];
    *(v2u*)(out + idx) = (v2u){pack2(y[0], y[1]), pack2(y[2], y[3])};
}

extern "C" void kernel_launch(void* const* d_in, const int* in_sizes, int n_in, void* d_out, int out_size, void* d_ws, size_t ws_size,
                              hipStream_t stream) {
    const u16* x = (const u16*)d_in[0];
    const u16* mem0 = (const u16*)d_in[1];
    const u16* Wq = (const u16*)d_in[2];
    const u16* Wk = (const u16*)d_in[3];
    const u16* Wv = (const u16*)d_in[4];
    const u16* Wo = (const u16*)d_in[5];
    const u16* Wg = (const u16*)d_in[8];
    const u16* Wu = (const u16*)d_in[9];
    const u16* Wd = (const u16*)d_in[10];
    char* ws = (char*)d_ws;
    (void)hipFuncSetAttribute((const void*)k_chain, hipFuncAttributeMaxDynamicSharedMemorySize, L_TOTAL);
    k_prep<<<PREP_LN_BLOCKS + PREP_TILE_BLOCKS + PREP_ZERO_BLOCKS, 256, 0, stream>>>(x, Wq, Wk, Wv, Wo, Wg, Wu, Wd, ws);
    k_chain<<<NCH + NWORK, 256, L_TOTAL, stream>>>(mem0, (const bf16x8*)(ws + OFF_WT), (const bf16x8*)(ws + OFF_WOT), (u16*)(ws + OFF_KM), (u16*)(ws + OFF_VM), (unsigned*)(ws + OFF_P),
                                                  (unsigned*)(ws + OFF_M), (unsigned*)(ws + OFF_TMO), (const bf16x8*)(ws + OFF_XN),
                                                  (const bf16x8*)(ws + OFF_WG), (const bf16x8*)(ws + OFF_WU), (const bf16x8*)(ws + OFF_WD),
                                                  (u16*)(ws + OFF_QX), (u16*)(ws + OFF_H), Wg, Wu, Wd, ws, Wq, Wk, Wv, Wo);
    k_post<<<256, 512, 0, stream>>>((u16*)(ws + OFF_QX), (u16*)(ws + OFF_KM), (const bf16x8*)(ws + OFF_WOT), (f32x4*)(ws + OFF_PART));
    k_out<<<S_ * DM / 4 / 256, 256, 0, stream>>>((const f32x4*)(ws + OFF_PART), (const u16*)(ws + OFF_H), x, (u16*)d_out);
}
```

```cpp
#include <hip/hip_runtime.h>
#include <hip/hip_bf16.h>

constexpr int S_ = 2048, DM = 512, NH = 32, DH = 128, MT = 8, SEG = 32, NSEG = 64, FF = 1792;

typedef unsigned short u16;
typedef __attribute__((ext_vector_type(8))) short bf16x8;
typedef __attribute__((ext_vector_type(4))) float f32x4;
typedef __attribute__((ext_vector_type(4))) unsigned v4u;
typedef __attribute__((ext_vector_type(2))) unsigned v2u;
typedef __bf16 bf16x2_t __attribute__((ext_vector_type(2)));

__device__ __forceinline__ unsigned pack2(float a, float b) {
    bf16x2_t v = {(__bf16)a, (__bf16)b};
    return __builtin_bit_cast(unsigned, v);
}
__device__ __forceinline__ float rbf(float a) { return (float)(__bf16)a; }
__device__ __forceinline__ float bf2f(u16 b) { return __uint_as_float(((unsigned)b) << 16); }
__device__ __forceinline__ bf16x8 mk8(unsigned a, unsigned b, unsigned c, unsigned d) {
    v4u v = {a, b, c, d};
    return __builtin_bit_cast(bf16x8, v);
}
#define MFMA(a, b, c) __builtin_amdgcn_mfma_f32_16x16x32_bf16((a), (b), (c), 0, 0, 0)

__device__ __forceinline__ int pi_col(int tau, int rho) { return 32 * (tau >> 1) + 8 * (rho >> 2) + 4 * (tau & 1) + (rho & 3); }

constexpr size_t OFF_TMO = 0;
constexpr size_t OFF_M   = 65536;
constexpr size_t OFF_P   = OFF_M + 64 * 8192;
constexpr size_t ZERO_BYTES = OFF_P + (size_t)63 * 32 * 16384;
constexpr size_t OFF_XN  = (ZERO_BYTES + 4095) / 4096 * 4096;
constexpr size_t OFF_WT  = OFF_XN + (size_t)S_ * DM * 2;
constexpr size_t OFF_WOT = OFF_WT + (size_t)3 * DM * NH * DH * 2;
constexpr size_t OFF_WG  = OFF_WOT + (size_t)DM * NH * DH * 2;
constexpr size_t OFF_WU  = OFF_WG + (size_t)DM * FF * 2;
constexpr size_t OFF_WD  = OFF_WU + (size_t)DM * FF * 2;
constexpr size_t OFF_QX  = OFF_WD + (size_t)DM * FF * 2;
constexpr size_t OFF_KX  = OFF_QX + (size_t)S_ * NH * DH * 2;
constexpr size_t OFF_VXT = OFF_KX + (size_t)S_ * NH * DH * 2;
constexpr size_t OFF_KM  = OFF_VXT + (size_t)S_ * NH * DH * 2;
constexpr size_t OFF_VM  = OFF_KM + (size_t)NSEG * NH * MT * DH * 2;
constexpr size_t OFF_H   = OFF_VM + (size_t)NSEG * NH * MT * DH * 2;
constexpr size_t OFF_O   = OFF_H + (size_t)S_ * FF * 2;
constexpr size_t OFF_PART = OFF_O + (size_t)S_ * NH * DH * 2;
constexpr size_t WS_TOTAL = OFF_PART + (size_t)8 * S_ * DM * 4;

__device__ __forceinline__ void ln_row(const u16* __restrict__ x, u16* __restrict__ xn_ft, int row, int lane) {
    const v4u raw = *(const v4u*)(x + (size_t)row * DM + lane * 8);
    float v[8];
    v[0] = __uint_as_float(raw.x << 16); v[1] = __uint_as_float(raw.x & 0xffff0000u);
    v[2] = __uint_as_float(raw.y << 16); v[3] = __uint_as_float(raw.y & 0xffff0000u);
    v[4] = __uint_as_float(raw.z << 16); v[5] = __uint_as_float(raw.z & 0xffff0000u);
    v[6] = __uint_as_float(raw.w << 16); v[7] = __uint_as_float(raw.w & 0xffff0000u);
    float s = 0.f;
#pragma unroll
    for (int j = 0; j < 8; ++j) s += v[j];
#pragma unroll
    for (int o = 1; o < 64; o <<= 1) s += __shfl_xor(s, o);
    const float mean = s * (1.0f / DM);
    float q = 0.f;
#pragma unroll
    for (int j = 0; j < 8; ++j) { const float d = v[j] - mean; q += d * d; }
#pragma unroll
    for (int o = 1; o < 64; o <<= 1) q += __shfl_xor(q, o);
    const float mb = rbf(mean), vb = rbf(q * (1.0f / DM));
    const float den = rbf(sqrtf(rbf(vb + 1e-05f)));
    float y[8];
#pragma unroll
    for (int j = 0; j < 8; ++j) y[j] = rbf(v[j] - mb) / den;
    v4u o4 = {pack2(y[0], y[1]), pack2(y[2], y[3]), pack2(y[4], y[5]), pack2(y[6], y[7])};
    const int mt = row >> 4, kk = lane >> 2, fl = (lane & 3) * 16 + (row & 15);
    *(v4u*)(xn_ft + (((size_t)mt * 16 + kk) * 64 + fl) * 8) = o4;
}

__device__ __forceinline__ void transpose8x8(const v4u (&r)[8], v4u (&o)[8]) {
#pragma unroll
    for (int c = 0; c < 4; ++c) {
#pragma unroll
        for (int d = 0; d < 4; ++d) {
            o[2 * c][d] = __builtin_amdgcn_perm(r[2 * d + 1][c], r[2 * d][c], 0x05040100u);
            o[2 * c + 1][d] = __builtin_amdgcn_perm(r[2 * d + 1][c], r[2 * d][c], 0x07060302u);
        }
    }
}
constexpr int B_WT = 3 * 64 * 512, B_WOT = 512 * 64, B_WG = 64 * 224, B_WD = 224 * 64;
constexpr int TILE_A = B_WT + B_WOT, TILE_BLOCKS = TILE_A + 2 * B_WG + B_WD;
__device__ __forceinline__ void tile_w_block(int c, const u16* __restrict__ Wq, const u16* __restrict__ Wk, const u16* __restrict__ Wv,
                                             const u16* __restrict__ Wo, const u16* __restrict__ Wg, const u16* __restrict__ Wu,
                                             const u16* __restrict__ Wd, char* ws, bool wt) {
    const u16* src; int pitch; u16* d0; int dstep;
    bool perm;
    if (c < B_WT) {
        const int nb = c & 511, kb = (c >> 9) & 63, m = c >> 15;
        const int h = nb >> 4, ks = (nb >> 2) & 3, gp = nb & 3, kk = kb >> 2, g = kb & 3;
        src = (m == 0 ? Wq : (m == 1 ? Wk : Wv)) + (size_t)(8 * kb) * 4096 + 8 * nb; pitch = 4096;
        d0 = (u16*)(ws + OFF_WT) + ((size_t)((h * 4 + ks) * 96 + m * 32 + kk) * 64 + g * 16 + 4 * gp) * 8; dstep = 16 * 64 * 8; perm = true;
    } else if ((c -= B_WT) < B_WOT) {
        const int nb = c & 63, kb = c >> 6, h = kb >> 4, ks = (kb >> 2) & 3, g = kb & 3, T = nb >> 1;
        src = Wo + (size_t)(8 * kb) * 512 + 8 * nb; pitch = 512;
        d0 = (u16*)(ws + OFF_WOT) + ((size_t)((h * 32 + T) * 4 + ks) * 64 + g * 16 + 8 * (nb & 1)) * 8; dstep = 0; perm = false;
    } else if ((c -= B_WOT) < 2 * B_WG) {
        const int which = c >= B_WG; if (which) c -= B_WG;
        const int nb = c % 224, kb = c / 224, fp = nb >> 2, gp = nb & 3, kk = kb >> 2, g = kb & 3;
        src = (which ? Wu : Wg) + (size_t)(8 * kb) * FF + 8 * nb; pitch = FF;
        d0 = (u16*)(ws + (which ? OFF_WU : OFF_WG)) + ((size_t)((2 * fp) * 16 + kk) * 64 + g * 16 + 4 * gp) * 8; dstep = 16 * 64 * 8; perm = true;
    } else {
        c -= 2 * B_WG;
        const int nb = c & 63, kb = c >> 6, kk = kb >> 2, g = kb & 3, nt = nb >> 1;
        src = Wd + (size_t)(8 * kb) * 512 + 8 * nb; pitch = 512;
        d0 = (u16*)(ws + OFF_WD) + ((size_t)(nt * 56 + kk) * 64 + g * 16 + 8 * (nb & 1)) * 8; dstep = 0; perm = false;
    }
    v4u r[8], o[8];
#pragma unroll
    for (int j = 0; j < 8; ++j) r[j] = *(const v4u*)(src + (size_t)j * pitch);
    transpose8x8(r, o);
    if (wt) {
        const __amdgpu_buffer_rsrc_t wrs = __builtin_amdgcn_make_buffer_rsrc(ws, 0, (unsigned)WS_TOTAL, 0x00020000);
        const unsigned b0 = (unsigned)((char*)d0 - ws);
#pragma unroll
        for (int j = 0; j < 8; ++j) __builtin_amdgcn_raw_buffer_store_b128(o[j], wrs, b0 + (unsigned)(perm ? (j >> 2) * dstep + (j & 3) * 8 : j * 8) * 2u, 0, 16);
    } else {
#pragma unroll
        for (int j = 0; j < 8; ++j) *(v4u*)(perm ? d0 + (j >> 2) * dstep + (j & 3) * 8 : d0 + j * 8) = o[j];
    }
}

constexpr int PREP_LN_BLOCKS = S_ / 4, PREP_TILE_BLOCKS = 65536 / 256, PREP_ZERO_BLOCKS = 256;
constexpr int ZSTEPS = 12;
constexpr size_t ZERO_A = OFF_P + (size_t)ZSTEPS * 32 * 16384;
__global__ __launch_bounds__(256) void k_prep(const u16* __restrict__ x, const u16* __restrict__ Wq, const u16* __restrict__ Wk,
                                              const u16* __restrict__ Wv, const u16* __restrict__ Wo, const u16* __restrict__ Wg,
                                              const u16* __restrict__ Wu, const u16* __restrict__ Wd, char* __restrict__ ws) {
    if (blockIdx.x < PREP_LN_BLOCKS) { ln_row(x, (u16*)(ws + OFF_XN), blockIdx.x * 4 + (threadIdx.x >> 6), threadIdx.x & 63); return; }
    if (blockIdx.x < PREP_LN_BLOCKS + PREP_TILE_BLOCKS) {
        const int c = (blockIdx.x - PREP_LN_BLOCKS) * 256 + threadIdx.x;
        tile_w_block(32768 + c, Wq, Wk, Wv, Wo, Wg, Wu, Wd, ws, false);
        return;
    }
    v4u* z = (v4u*)ws;
    for (size_t e = (size_t)(blockIdx.x - PREP_LN_BLOCKS - PREP_TILE_BLOCKS) * 256 + threadIdx.x; e < ZERO_A / 16; e += (size_t)PREP_ZERO_BLOCKS * 256)
        z[e] = (v4u){0u, 0u, 0u, 0u};
}

#define GLDS16(gp, lp) __builtin_amdgcn_global_load_lds((const unsigned*)(gp), (unsigned*)(lp), 16, 0, 0)
constexpr unsigned QKV_MAT_BYTES = (unsigned)S_ * NH * DH * 2;
__device__ __forceinline__ void qkv_tile(char* lds, const bf16x8* __restrict__ XN, const bf16x8* WT, __amdgpu_buffer_rsrc_t qrs,
                                         int mtile, int m, int h, int w, int lane) {
    bf16x8 (*sm)[32][64] = (bf16x8 (*)[32][64])lds;
    const int wm = w >> 1, wn = w & 1, g = lane >> 4, i = lane & 15;
    const bf16x8* sp = w < 2 ? XN + ((size_t)(mtile * 8 + w * 4) * 16) * 64 + lane
                             : WT + ((size_t)(h * 4 + (w - 2) * 2) * 96 + m * 32) * 64 + lane;
    auto stage = [&](int c, int buf) {
#pragma unroll
        for (int f = 0; f < 8; ++f) {
            const int t = f >> 1, kk = 2 * c + (f & 1);
            const int off = w < 2 ? (t * 16 + kk) * 64 : ((t >> 1) * 96 + (t & 1) * 16 + kk) * 64;
            GLDS16(sp + off, &sm[buf][w * 8 + f][0]);
        }
    };
    f32x4 acc[4][4];
#pragma unroll
    for (int a = 0; a < 4; ++a)
#pragma unroll
        for (int b = 0; b < 4; ++b) acc[a][b] = (f32x4){0.f, 0.f, 0.f, 0.f};
    __syncthreads();
    stage(0, 0);
    for (int c = 0; c < 8; ++c) {
        asm volatile("s_waitcnt vmcnt(0)" ::: "memory");
        __syncthreads();
        if (c + 1 < 8) stage(c + 1, (c + 1) & 1);
#pragma unroll
        for (int k2 = 0; k2 < 2; ++k2) {
            bf16x8 a[4], b[4];
#pragma unroll
            for (int t = 0; t < 4; ++t) { a[t] = sm[c & 1][(wm * 4 + t) * 2 + k2][lane]; b[t] = sm[c & 1][16 + (wn * 4 + t) * 2 + k2][lane]; }
            if (m < 2) {
#pragma unroll
                for (int mt = 0; mt < 4; ++mt)
#pragma unroll
                    for (int t = 0; t < 4; ++t) acc[mt][t] = MFMA(b[t], a[mt], acc[mt][t]);
            } else {
#pragma unroll
                for (int mt = 0; mt < 4; ++mt)
#pragma unroll
                    for (int t = 0; t < 4; ++t) acc[mt][t] = MFMA(a[mt], b[t], acc[mt][t]);
            }
        }
    }
    const int mb = mtile * 2 + wm;
    if (m < 2) {
#pragma unroll
        for (int mt = 0; mt < 4; ++mt) {
            const int tok = mb * 64 + mt * 16 + i, s = tok >> 5, tl = tok & 31;
            const int tile = m == 0 ? (tl >> 4) : ((tl >> 2) & 1), row = m == 0 ? (tl & 15) : (4 * (tl >> 3) + (tl & 3));
#pragma unroll
            for (int p = 0; p < 2; ++p) {
                const int ks = 2 * wn + p;
                v4u o = {pack2(acc[mt][2 * p][0], acc[mt][2 * p][1]), pack2(acc[mt][2 * p][2], acc[mt][2 * p][3]),
                         pack2(acc[mt][2 * p + 1][0], acc[mt][2 * p + 1][1]), pack2(acc[mt][2 * p + 1][2], acc[mt][2 * p + 1][3])};
                __builtin_amdgcn_raw_buffer_store_b128(o, qrs, (unsigned)m * QKV_MAT_BYTES + (unsigned)((s * NH + h) * 8192 + ((tile * 4 + ks) * 64 + g * 16 + row) * 16), 0, 16);
            }
        }
    } else {
#pragma unroll
        for (int mt = 0; mt < 4; ++mt) {
            const int s = mb * 2 + (mt >> 1), gp = 2 * (mt & 1) + (g >> 1);
#pragma unroll
            for (int t = 0; t < 4; ++t) {
                const int tau = 4 * wn + t;
                v2u o = {pack2(acc[mt][t][0], acc[mt][t][1]), pack2(acc[mt][t][2], acc[mt][t][3])};
                __builtin_amdgcn_raw_buffer_store_b64(o, qrs, 2u * QKV_MAT_BYTES + (unsigned)((s * NH + h) * 8192 + (tau * 64 + gp * 16 + i) * 16 + (g & 1) * 8), 0, 16);
            }
        }
    }
}

__device__ __forceinline__ void glu_block(const bf16x8* __restrict__ XN, const bf16x8* WG, const bf16x8* WU,
                                          const bf16x8* WD, u16* __restrict__ Bout, char* lds, int mb, int w, int lane) {
    const int g = lane >> 4, i = lane & 15;
    bf16x8* Hl = (bf16x8*)lds;
    const bf16x8* xa = XN + (size_t)(mb * 2) * 16 * 64 + lane;
    for (int fp = w; fp < 56; fp += 4) {
        f32x4 ag[2][2], au[2][2];
#pragma unroll
        for (int a = 0; a < 2; ++a)
#pragma unroll
            for (int b = 0; b < 2; ++b) { ag[a][b] = (f32x4){0.f, 0.f, 0.f, 0.f}; au[a][b] = (f32x4){0.f, 0.f, 0.f, 0.f}; }
        const bf16x8* wg = WG + (size_t)(fp * 2) * 16 * 64 + lane;
        const bf16x8* wu = WU + (size_t)(fp * 2) * 16 * 64 + lane;
#pragma unroll 4
        for (int kk = 0; kk < 16; ++kk) {
            bf16x8 a[2], bg[2], bu[2];
#pragma unroll
            for (int t = 0; t < 2; ++t) { a[t] = xa[(t * 16 + kk) * 64]; bg[t] = wg[(t * 16 + kk) * 64]; bu[t] = wu[(t * 16 + kk) * 64]; }
#pragma unroll
            for (int mt = 0; mt < 2; ++mt)
#pragma unroll
                for (int t = 0; t < 2; ++t) { ag[mt][t] = MFMA(bg[t], a[mt], ag[mt][t]); au[mt][t] = MFMA(bu[t], a[mt], au[mt][t]); }
        }
#pragma unroll
        for (int mt = 0; mt < 2; ++mt) {
            float hv[8];
#pragma unroll
            for (int t = 0; t < 2; ++t)
#pragma unroll
                for (int r = 0; r < 4; ++r) {
                    const float gb = rbf(ag[mt][t][r]), ub = rbf(au[mt][t][r]);
                    const float sg = rbf(1.0f / (1.0f + __expf(-gb)));
                    hv[t * 4 + r] = sg * ub;
                }
            Hl[(mt * 56 + fp) * 64 + lane] = mk8(pack2(hv[0], hv[1]), pack2(hv[2], hv[3]), pack2(hv[4], hv[5]), pack2(hv[6], hv[7]));
        }
    }
    __syncthreads();
    for (int nq = w; nq < 8; nq += 4) {
        f32x4 ab[2][4];
#pragma unroll
        for (int a = 0; a < 2; ++a)
#pragma unroll
            for (int b = 0; b < 4; ++b) ab[a][b] = (f32x4){0.f, 0.f, 0.f, 0.f};
        const bf16x8* pb = WD + (size_t)(nq * 4) * 56 * 64 + lane;
#pragma unroll 4
        for (int kk = 0; kk < 56; ++kk) {
            const bf16x8 a0 = Hl[kk * 64 + lane], a1 = Hl[(56 + kk) * 64 + lane];
#pragma unroll
            for (int t = 0; t < 4; ++t) {
                const bf16x8 b = pb[(t * 56 + kk) * 64];
                ab[0][t] = MFMA(b, a0, ab[0][t]); ab[1][t] = MFMA(b, a1, ab[1][t]);
            }
        }
#pragma unroll
        for (int mt = 0; mt < 2; ++mt)
#pragma unroll
            for (int t = 0; t < 4; ++t)
                *(v2u*)(Bout + (size_t)(mb * 32 + mt * 16 + i) * DM + (nq * 4 + t) * 16 + 4 * g) =
                    (v2u){pack2(ab[mt][t][0], ab[mt][t][1]), pack2(ab[mt][t][2], ab[mt][t][3])};
    }
}

constexpr int L_WO = 0, L_MEM = 131072, L_SC = L_MEM + 8192, L_OE = L_SC + 6144, L_PX = L_OE + 2048, L_TOTAL = L_PX + 4 * 640;
constexpr unsigned SPINCAP = 1u << 20;
#define JS 4
constexpr int NCH = NH * JS;
constexpr int NWORK = 256 - NCH;
constexpr int TPW = 8 / JS;
constexpr float SCALE = 0.08838834764831845f;
#define SLP1 10
#define AUXS 16
#define AUXL 16
#define SLPN 23
#define SLP2 12

__device__ __forceinline__ float xor32_max(float x) { auto r = __builtin_amdgcn_permlane32_swap(__float_as_uint(x), __float_as_uint(x), false, false); return fmaxf(__uint_as_float(r[0]), __uint_as_float(r[1])); }
__device__ __forceinline__ float xor16_max(float x) { auto r = __builtin_amdgcn_permlane16_swap(__float_as_uint(x), __float_as_uint(x), false, false); return fmaxf(__uint_as_float(r[0]), __uint_as_float(r[1])); }
__device__ __forceinline__ float xor32_add(float x) { auto r = __builtin_amdgcn_permlane32_swap(__float_as_uint(x), __float_as_uint(x), false, false); return __uint_as_float(r[0]) + __uint_as_float(r[1]); }
__device__ __forceinline__ float xor16_add(float x) { auto r = __builtin_amdgcn_permlane16_swap(__float_as_uint(x), __float_as_uint(x), false, false); return __uint_as_float(r[0]) + __uint_as_float(r[1]); }
template <int CTRL> __device__ __forceinline__ float dpp_f(float x) { return __int_as_float(__builtin_amdgcn_update_dpp(0, __float_as_int(x), CTRL, 0xf, 0xf, false)); }
__device__ __forceinline__ float ror8(float x) { return __int_as_float(__builtin_amdgcn_update_dpp(0, __float_as_int(x), 0x128, 0xf, 0xf, false)); }

typedef __attribute__((ext_vector_type(2))) float f32x2;
__device__ __forceinline__ unsigned cvtpk(float a, float b) { unsigned r; asm("v_cvt_pk_bf16_f32 %0, %1, %2" : "=v"(r) : "v"(a), "v"(b)); return r; }
__device__ __forceinline__ float vmax(float a, float b) { float r; asm("v_max_f32 %0, %1, %2" : "=v"(r) : "v"(a), "v"(b)); return r; }
__device__ __forceinline__ float vmax3(float a, float b, float c) { float r; asm("v_max3_f32 %0, %1, %2, %3" : "=v"(r) : "v"(a), "v"(b), "v"(c)); return r; }
__device__ __forceinline__ float xmax32(float x) { auto r = __builtin_amdgcn_permlane32_swap(__float_as_uint(x), __float_as_uint(x), false, false); return vmax(__uint_as_float(r[0]), __uint_as_float(r[1])); }
__device__ __forceinline__ float xmax16(float x) { auto r = __builtin_amdgcn_permlane16_swap(__float_as_uint(x), __float_as_uint(x), false, false); return vmax(__uint_as_float(r[0]), __uint_as_float(r[1])); }
constexpr unsigned KM_BYTES = (unsigned)NSEG * NH * MT * DH * 2;
__device__ __forceinline__ void xattn_unit(__amdgpu_buffer_rsrc_t qrs, __amdgpu_buffer_rsrc_t krs, bf16x8* dst, int s, int h, int lane) {
    const int g = lane >> 4, i = lane & 15;
    const unsigned qo = (unsigned)((s * NH + h) * 8192 + lane * 16), mo = (unsigned)((s * NH + h) * 2048);
#define LDQ(off) __builtin_bit_cast(bf16x8, __builtin_amdgcn_raw_buffer_load_b128(qrs, (off), 0, 0))
    bf16x8 kf[4], k1[4], k2[4], qf[2][4], vx[8]; v2u vm[8];
#pragma unroll
    for (int ks = 0; ks < 4; ++ks) {
        kf[ks] = __builtin_bit_cast(bf16x8, __builtin_amdgcn_raw_buffer_load_b128(krs, mo + (unsigned)((i & 7) * 256 + 64 * ks + 16 * g), 0, 0));
        k1[ks] = LDQ(QKV_MAT_BYTES + qo + ks * 1024);
        k2[ks] = LDQ(QKV_MAT_BYTES + qo + (4 + ks) * 1024);
        qf[0][ks] = LDQ(qo + ks * 1024);
        qf[1][ks] = LDQ(qo + (4 + ks) * 1024);
    }
#pragma unroll
    for (int tau = 0; tau < 8; ++tau) {
        vx[tau] = LDQ(2u * QKV_MAT_BYTES + qo + tau * 1024);
        vm[tau] = __builtin_amdgcn_raw_buffer_load_b64(krs, KM_BYTES + mo + (unsigned)((tau * 32 + (lane & 31)) * 8), 0, 0);
    }
#pragma unroll
    for (int qt = 0; qt < 2; ++qt) {
        f32x4 sc[3];
#pragma unroll
        for (int a = 0; a < 3; ++a) sc[a] = (f32x4){0.f, 0.f, 0.f, 0.f};
#pragma unroll
        for (int ks = 0; ks < 4; ++ks) {
            sc[0] = MFMA(kf[ks], qf[qt][ks], sc[0]);
            sc[1] = MFMA(k1[ks], qf[qt][ks], sc[1]);
            sc[2] = MFMA(k2[ks], qf[qt][ks], sc[2]);
        }
        const int tq = qt * 16 + i;
        float sv[12]; bool ok[12];
#pragma unroll
        for (int r = 0; r < 4; ++r) { ok[r] = g < 2; ok[4 + r] = (8 * g + r) <= tq; ok[8 + r] = (8 * g + 4 + r) <= tq; }
        float mx = -3.0e38f;
#pragma unroll
        for (int a = 0; a < 3; ++a)
#pragma unroll
            for (int r = 0; r < 4; ++r) { sv[a * 4 + r] = rbf(rbf(sc[a][r]) * SCALE); mx = ok[a * 4 + r] ? fmaxf(mx, sv[a * 4 + r]) : mx; }
        mx = xor16_max(mx);
        mx = xor32_max(mx);
        float ex[12], sum = 0.f;
#pragma unroll
        for (int a = 0; a < 12; ++a) { ex[a] = ok[a] ? __expf(sv[a] - mx) : 0.f; sum += ex[a]; }
        sum = xor16_add(sum);
        sum = xor32_add(sum);
        const float inv = 1.0f / sum;
        const bf16x8 pX = mk8(pack2(ex[4] * inv, ex[5] * inv), pack2(ex[6] * inv, ex[7] * inv), pack2(ex[8] * inv, ex[9] * inv), pack2(ex[10] * inv, ex[11] * inv));
        const bf16x8 pM = mk8(pack2(ex[0] * inv, ex[1] * inv), pack2(ex[2] * inv, ex[3] * inv), 0u, 0u);
#pragma unroll
        for (int ks = 0; ks < 4; ++ks) {
            f32x4 o[2];
#pragma unroll
            for (int t = 0; t < 2; ++t) {
                const int tau = 2 * ks + t;
                o[t] = MFMA(vx[tau], pX, ((f32x4){0.f, 0.f, 0.f, 0.f}));
                o[t] = MFMA(mk8(vm[tau].x, vm[tau].y, 0u, 0u), pM, o[t]);
            }
            dst[(qt * 16 + ks) * 64 + lane] =
                mk8(pack2(o[0][0], o[0][1]), pack2(o[0][2], o[0][3]), pack2(o[1][0], o[1][1]), pack2(o[1][2], o[1][3]));
        }
    }
}

#define AMFMA0(d, a, b) asm volatile("s_nop 1\n\tv_mfma_f32_16x16x32_bf16 %0, %1, %2, 0" : "=&v"(d) : "v"(a), "v"(b))
#define AMFMA(d, a, b) asm volatile("s_nop 1\n\tv_mfma_f32_16x16x32_bf16 %0, %1, %2, %0" : "+v"(d) : "v"(a), "v"(b))
#define AMFMA16(d, a, b) asm volatile("s_nop 1\n\tv_mfma_f32_16x16x16_bf16 %0, %1, %2, %0" : "+v"(d) : "v"(a), "v"(b))

__global__ __launch_bounds__(256, 1) void k_chain(const u16* __restrict__ mem0, const bf16x8* WT, const bf16x8* WOT,
                                                  u16* __restrict__ KM, u16* __restrict__ VM, unsigned* Pbuf, unsigned* Mbuf, unsigned* tmo,
                                                  const bf16x8* __restrict__ XN, const bf16x8* WG, const bf16x8* WU,
                                                  const bf16x8* WD, u16* QX, u16* __restrict__ Bout,
                                                  const u16* __restrict__ Wg, const u16* __restrict__ Wu, const u16* __restrict__ Wd, char* ws,
                                                  const u16* __restrict__ Wq, const u16* __restrict__ Wk, const u16* __restrict__ Wv,
                                                  const u16* __restrict__ Wo) {
    extern __shared__ __attribute__((aligned(16))) char lds[];
    const int h = blockIdx.x & 31, jj = blockIdx.x >> 5;
    const int w = __builtin_amdgcn_readfirstlane(threadIdx.x >> 6), lane = threadIdx.x & 63, g = lane >> 4, i = lane & 15;
    const __amdgpu_buffer_rsrc_t qrs = __builtin_amdgcn_make_buffer_rsrc(QX, 0, 3u * QKV_MAT_BYTES, 0x00020000);
    const __amdgpu_buffer_rsrc_t krs = __builtin_amdgcn_make_buffer_rsrc(KM, 0, 2u * KM_BYTES, 0x00020000);
    unsigned* kvcnt = tmo + 16;
    unsigned* zcnt = tmo + 64;
    if (blockIdx.x >= NCH) {
        const int wb = blockIdx.x - NCH;
        for (int t = wb; t < 1024; t += NWORK) {
            qkv_tile(lds, XN, WT, qrs, t >> 6, 1 + ((t >> 5) & 1), t & 31, w, lane);
            asm volatile("s_waitcnt vmcnt(0)" ::: "memory");
            __syncthreads();
            if (threadIdx.x == 0) __hip_atomic_fetch_add(kvcnt + (t >> 6), 1u, __ATOMIC_RELAXED, __HIP_MEMORY_SCOPE_AGENT);
            if (t == wb)
            {
                const __amdgpu_buffer_rsrc_t zrs = __builtin_amdgcn_make_buffer_rsrc(Pbuf, 0, 63 * 32 * 16384, 0x00020000);
                for (unsigned e = (unsigned)ZSTEPS * 32768u + (unsigned)(wb * 256 + threadIdx.x); e < 63u * 32768u; e += (unsigned)NWORK * 256u)
                    __builtin_amdgcn_raw_buffer_store_b128((v4u){0u, 0u, 0u, 0u}, zrs, e * 16u, 0, 16);
                static_assert(NWORK == 128, "work split");
                tile_w_block(wb * 256 + threadIdx.x, Wq, Wk, Wv, Wo, Wg, Wu, Wd, ws, true);
#pragma unroll 2
                for (int c = B_WT + wb * 256 + threadIdx.x; c < TILE_BLOCKS; c += NWORK * 256) tile_w_block(c, Wq, Wk, Wv, Wo, Wg, Wu, Wd, ws, true);
                asm volatile("s_waitcnt vmcnt(0)" ::: "memory");
                __syncthreads();
                if (threadIdx.x == 0) __hip_atomic_fetch_add(zcnt, 1u, __ATOMIC_RELAXED, __HIP_MEMORY_SCOPE_AGENT);
            }
        }
        __syncthreads();
        {
            if (threadIdx.x == 0) {
                unsigned spins = 0;
                while (__hip_atomic_load(zcnt, __ATOMIC_RELAXED, __HIP_MEMORY_SCOPE_AGENT) < (unsigned)NWORK) {
                    __builtin_amdgcn_s_sleep(8);
                    if (++spins > SPINCAP) { atomicAdd(tmo, 1u); break; }
                }
                __builtin_amdgcn_fence(__ATOMIC_ACQUIRE, "agent");
                asm volatile("s_waitcnt vmcnt(0)" ::: "memory");
            }
            __syncthreads();
        }
        if (wb < 64) {
            glu_block(XN, WG, WU, WD, Bout, lds, wb, w, lane);
        } else {
            for (int t = wb - 64; t < 512; t += NWORK - 64) {
                qkv_tile(lds, XN, WT, qrs, t >> 5, 0, t & 31, w, lane);
            }
        }
        return;
    }
    unsigned kv_ready = 0;
    auto wait_kv = [&](int grp) {
        unsigned spins = 0;
        while (!((kv_ready >> grp) & 1u)) {
            const unsigned c = __hip_atomic_load(kvcnt + (lane & 15), __ATOMIC_RELAXED, __HIP_MEMORY_SCOPE_AGENT);
            kv_ready = (unsigned)__ballot(c >= 64u) & 0xffffu;
            if ((kv_ready >> grp) & 1u) break;
            __builtin_amdgcn_s_sleep(2);
            if (++spins > SPINCAP) { if (lane == 0) atomicAdd(tmo, 1u); break; }
        }
    };
    auto fetch_kv = [&](int sg, v4u (&pk)[2], v4u (&pv)[2]) {
        const unsigned o = (unsigned)((sg * NH + h) * 8192 + lane * 16);
        pk[0] = __builtin_amdgcn_raw_buffer_load_b128(qrs, QKV_MAT_BYTES + o + w * 1024, 0, 16);
        pk[1] = __builtin_amdgcn_raw_buffer_load_b128(qrs, QKV_MAT_BYTES + o + w * 1024 + 4096, 0, 16);
        pv[0] = __builtin_amdgcn_raw_buffer_load_b128(qrs, 2u * QKV_MAT_BYTES + o + w * 2048, 0, 16);
        pv[1] = __builtin_amdgcn_raw_buffer_load_b128(qrs, 2u * QKV_MAT_BYTES + o + w * 2048 + 1024, 0, 16);
    };
    const __amdgpu_buffer_rsrc_t prs = __builtin_amdgcn_make_buffer_rsrc(Pbuf, 0, 63 * 32 * 16384, 0x00020000);
    const __amdgpu_buffer_rsrc_t mrs = __builtin_amdgcn_make_buffer_rsrc(Mbuf, 0, 64 * 8192, 0x00020000);

    static_assert(JS == 4, "the Wo self-load below assumes 2 output tiles per wave");
    bf16x8 WA[64], WV[32];
    {
        char* stg = lds + 32768 + w * 16384;
        const int cb = lane & 3;
#pragma unroll
        for (int ps = 0; ps < 6; ++ps) {
            const int m = ps >> 1, half = ps & 1;
            const u16* Wm = m == 0 ? Wq : (m == 1 ? Wk : Wv);
#pragma unroll
            for (int it = 0; it < 2; ++it) {
                const int kbl = (lane >> 2) + 16 * it;
                const u16* sp = Wm + (size_t)(8 * (half * 32 + kbl)) * 4096 + h * DH + 32 * w + 8 * cb;
                v4u r[8], o[8];
#pragma unroll
                for (int j = 0; j < 8; ++j) r[j] = *(const v4u*)(sp + (size_t)j * 4096);
                transpose8x8(r, o);
                char* d = stg + (kbl >> 2) * 1024 + ((kbl & 3) * 16 + 4 * cb) * 16;
#pragma unroll
                for (int j = 0; j < 8; ++j) *(v4u*)(d + (j >> 2) * 8192 + (j & 3) * 16) = o[j];
            }
            __builtin_amdgcn_wave_barrier();
#pragma unroll
            for (int f = 0; f < 16; ++f) {
                const bf16x8 v = *(const bf16x8*)(stg + f * 1024 + lane * 16);
                const int idx = (f >> 3) * 16 + half * 8 + (f & 7);
                if (m < 2) WA[m * 32 + idx] = v; else WV[idx] = v;
            }
            asm volatile("s_waitcnt lgkmcnt(0)" ::: "memory");
            __builtin_amdgcn_wave_barrier();
        }
        {
            const int kb = lane >> 2;
            const u16* sp = Wo + (size_t)(h * DH + 8 * kb) * DM + 32 * (jj * 4 + w) + 8 * cb;
            v4u r[8], o[8];
#pragma unroll
            for (int j = 0; j < 8; ++j) r[j] = *(const v4u*)(sp + (size_t)j * DM);
            transpose8x8(r, o);
            char* d = lds + L_WO + (w * 8 + (cb >> 1) * 4 + (kb >> 2)) * 1024 + ((kb & 3) * 16 + 8 * (cb & 1)) * 16;
#pragma unroll
            for (int j = 0; j < 8; ++j) *(v4u*)(d + j * 16) = o[j];
        }
    }
    {
        const int tid = threadIdx.x;
#pragma unroll
        for (int c = tid; c < 512; c += 256) {
            const int t = c & 7, gg = (c >> 3) & 3, kk = c >> 5;
            *(v4u*)(lds + L_MEM + c * 16) = *(const v4u*)(mem0 + t * DM + 32 * kk + 8 * gg);
        }
    }
    v4u pk[2], pv[2];
    wait_kv(0);
    fetch_kv(0, pk, pv);
    const int rid = blockIdx.x * 4 + w;
    const bool is_red = (rid % JS) == 0;
    const int red = rid / JS;
#define STAMP(k)
    for (int s = 0; s < NSEG; ++s) {
        __syncthreads();
        STAMP(0);
        asm volatile("" : "+v"(pk[0]), "+v"(pk[1]), "+v"(pv[0]), "+v"(pv[1]));
        f32x4 acc[6];
        bf16x8 mf[16];
        const char* mp = lds + L_MEM + g * 128 + (i & 7) * 16;
        {
#pragma unroll
            for (int kk = 0; kk < 4; ++kk) mf[kk] = *(const bf16x8*)(mp + kk * 512);
#pragma unroll
            for (int kk = 0; kk < 16; ++kk) {
                if (kk + 4 < 16) mf[kk + 4] = *(const bf16x8*)(mp + (kk + 4) * 512);
                if (kk == 0) {
                    asm volatile("v_mfma_f32_16x16x32_bf16 %0, %4, %8, 0\n\tv_mfma_f32_16x16x32_bf16 %1, %5, %8, 0\n\t"
                                 "v_mfma_f32_16x16x32_bf16 %2, %6, %8, 0\n\tv_mfma_f32_16x16x32_bf16 %3, %7, %8, 0"
                                 : "=&v"(acc[0]), "=&v"(acc[1]), "=&v"(acc[2]), "=&v"(acc[3])
                                 : "a"(WA[0]), "a"(WA[16]), "a"(WA[32]), "a"(WA[48]), "v"(mf[0]));
                } else {
                    asm volatile("v_mfma_f32_16x16x32_bf16 %0, %4, %8, %0\n\tv_mfma_f32_16x16x32_bf16 %1, %5, %8, %1\n\t"
                                 "v_mfma_f32_16x16x32_bf16 %2, %6, %8, %2\n\tv_mfma_f32_16x16x32_bf16 %3, %7, %8, %3"
                                 : "+v"(acc[0]), "+v"(acc[1]), "+v"(acc[2]), "+v"(acc[3])
                                 : "a"(WA[kk]), "a"(WA[16 + kk]), "a"(WA[32 + kk]), "a"(WA[48 + kk]), "v"(mf[kk]));
                }
            }
            asm volatile("s_nop 7\n\ts_nop 1" : "+v"(acc[0]), "+v"(acc[1]), "+v"(acc[2]), "+v"(acc[3]));
        }
        const bf16x8 qf = mk8(pack2(acc[0][0], acc[0][1]), pack2(acc[0][2], acc[0][3]), pack2(acc[1][0], acc[1][1]), pack2(acc[1][2], acc[1][3]));
        const bf16x8 kf = mk8(pack2(acc[2][0], acc[2][1]), pack2(acc[2][2], acc[2][3]), pack2(acc[3][0], acc[3][1]), pack2(acc[3][2], acc[3][3]));
        if (i < 8 && jj == 0) __builtin_amdgcn_raw_buffer_store_b128(__builtin_bit_cast(v4u, kf), krs, (unsigned)((s * NH + h) * 2048 + i * 256 + 64 * w + 16 * g), 0, 0);
        {
            f32x4 sp[3];
            AMFMA0(sp[0], kf, qf); AMFMA0(sp[1], pk[0], qf); AMFMA0(sp[2], pk[1], qf);
            asm volatile("s_nop 7" : "+v"(sp[0]), "+v"(sp[1]), "+v"(sp[2]));
            if (i < 8) {
#pragma unroll
                for (int a = 0; a < 3; ++a) *(f32x4*)(lds + L_SC + (((w * 3 + a) * 4 + g) * 8 + i) * 16) = sp[a];
            }
        }
#pragma unroll
        for (int kk = 0; kk < 4; ++kk) mf[kk] = *(const bf16x8*)(mp + kk * 512);
        STAMP(1);
        __syncthreads();
        STAMP(2);
#define VS(kk, hf)                                                                                                             \
        do {                                                                                                                   \
            if ((hf) == 0 && (kk) + 4 < 16) mf[((kk) + 4) & 15] = *(const bf16x8*)(mp + ((kk) + 4) * 512);                     \
            if ((kk) == 0) asm volatile("v_mfma_f32_16x16x32_bf16 %0, %2, %1, 0" : "=&v"(acc[4 + (hf)]) : "v"(WV[16 * (hf)]), "v"(mf[0]));      \
            else asm volatile("v_mfma_f32_16x16x32_bf16 %0, %2, %1, %0" : "+v"(acc[4 + (hf)]) : "v"(WV[16 * (hf) + (kk)]), "v"(mf[kk]));        \
        } while (0)
        v2u v0, v1;
        bf16x8 wf[4][8];
        const char* wp = lds + L_WO + (w * TPW * 4) * 1024 + lane * 16;
        {
            const int q = lane & 7, kg = lane >> 3;
            const char* sx = lds + L_SC + (((1 + (kg & 1)) * 4 + (kg >> 1)) * 8 + q) * 16;
            const char* sm_ = lds + L_SC + ((kg >> 2) * 8 + q) * 16 + (kg & 3) * 4;
            f32x4 px[4]; float pm[4];
#pragma unroll
            for (int ww = 0; ww < 4; ++ww) { px[ww] = *(const f32x4*)(sx + ww * 1536); pm[ww] = *(const float*)(sm_ + ww * 1536); }
            VS(0, 0); VS(0, 1); VS(1, 0); VS(1, 1); VS(2, 0); VS(2, 1); VS(3, 0); VS(3, 1);
            const f32x4 tx = (px[0] + px[1]) + (px[2] + px[3]);
            VS(4, 0);
            const float tm = (pm[0] + pm[1]) + (pm[2] + pm[3]);
            VS(4, 1);
            const unsigned u0 = cvtpk(tx[0], tx[1]), u1 = cvtpk(tx[2], tx[3]), u2 = cvtpk(tm, tm);
            VS(5, 0);
            const f32x4 bx = (f32x4){__uint_as_float(u0 << 16), __uint_as_float(u0 & 0xffff0000u), __uint_as_float(u1 << 16), __uint_as_float(u1 & 0xffff0000u)} * SCALE;
            const float bm = __uint_as_float(u2 << 16) * SCALE;
            VS(5, 1);
            const unsigned w0 = cvtpk(bx[0], bx[1]), w1 = cvtpk(bx[2], bx[3]), w2 = cvtpk(bm, bm);
            VS(6, 0);
            const float s0 = __uint_as_float(w0 << 16), s1 = __uint_as_float(w0 & 0xffff0000u), s2 = __uint_as_float(w1 << 16),
                        s3 = __uint_as_float(w1 & 0xffff0000u), s4 = __uint_as_float(w2 << 16);
            VS(6, 1);
            float mx = vmax3(vmax3(s0, s1, s2), s3, s4);
            VS(7, 0);
            mx = vmax(mx, ror8(mx)); VS(7, 1);
            mx = xmax16(mx); VS(8, 0);
            mx = xmax32(mx); VS(8, 1);
            constexpr float L2E = 1.4426950408889634f;
            const float nm = -mx * L2E;
            const float e0 = __builtin_amdgcn_exp2f(__builtin_fmaf(s0, L2E, nm)), e1 = __builtin_amdgcn_exp2f(__builtin_fmaf(s1, L2E, nm));
            VS(9, 0);
            const float e2 = __builtin_amdgcn_exp2f(__builtin_fmaf(s2, L2E, nm)), e3 = __builtin_amdgcn_exp2f(__builtin_fmaf(s3, L2E, nm));
            VS(9, 1);
            const float e4 = __builtin_amdgcn_exp2f(__builtin_fmaf(s4, L2E, nm));
            float sum = ((e0 + e1) + (e2 + e3)) + e4;
            VS(10, 0);
            sum += ror8(sum); VS(10, 1);
            sum = xor16_add(sum); VS(11, 0);
            sum = xor32_add(sum); VS(11, 1);
            const float inv = __builtin_amdgcn_rcpf(sum);
            char* pw = lds + L_PX + w * 640;
            *(v2u*)(pw + q * 64 + kg * 8) = (v2u){cvtpk(e0 * inv, e1 * inv), cvtpk(e2 * inv, e3 * inv)};
            *(u16*)(pw + 512 + q * 16 + kg * 2) = (u16)cvtpk(e4 * inv, e4 * inv);
            VS(12, 0); VS(12, 1); VS(13, 0); VS(13, 1);
            __builtin_amdgcn_wave_barrier();
            const bf16x8 pX = *(const bf16x8*)(pw + (i & 7) * 64 + g * 16);
            v2u pM = *(const v2u*)(pw + 512 + (i & 7) * 16 + (g & 1) * 8);
#pragma unroll
            for (int f = 0; f < 8; ++f) wf[0][f] = *(const bf16x8*)(wp + f * 1024);
            VS(14, 0); VS(14, 1); VS(15, 0); VS(15, 1);
            asm volatile("s_nop 7" : "+v"(acc[4]), "+v"(acc[5]));
            v0 = (v2u){cvtpk(acc[4][0], acc[4][1]), cvtpk(acc[4][2], acc[4][3])};
            v1 = (v2u){cvtpk(acc[5][0], acc[5][1]), cvtpk(acc[5][2], acc[5][3])};
            if (g >= 2) pM = (v2u){0u, 0u};
            f32x4 o0, o1;
            AMFMA0(o0, pv[0], pX); AMFMA0(o1, pv[1], pX);
            asm volatile("s_nop 5" : "+v"(o0), "+v"(o1));
            AMFMA16(o0, v0, pM); AMFMA16(o1, v1, pM);
            asm volatile("s_nop 7" : "+v"(o0), "+v"(o1));
            if (i < 8) *(bf16x8*)(lds + L_OE + ((w * 4 + g) * 8 + i) * 16) = mk8(cvtpk(o0[0], o0[1]), cvtpk(o0[2], o0[3]), cvtpk(o1[0], o1[1]), cvtpk(o1[2], o1[3]));
        }
        if (g < 2 && jj == 0) {
            __builtin_amdgcn_raw_buffer_store_b64(v0, krs, KM_BYTES + (unsigned)((s * NH + h) * 2048 + (2 * w * 32 + lane) * 8), 0, 0);
            __builtin_amdgcn_raw_buffer_store_b64(v1, krs, KM_BYTES + (unsigned)((s * NH + h) * 2048 + ((2 * w + 1) * 32 + lane) * 8), 0, 0);
        }
        if (s == NSEG - 1) break;
        const unsigned pofs = (unsigned)s * (32u * 16384u);
        if (s == ZSTEPS) {
            unsigned spins = 0;
            while ((unsigned)__builtin_amdgcn_readfirstlane(__hip_atomic_load(zcnt, __ATOMIC_RELAXED, __HIP_MEMORY_SCOPE_AGENT)) < (unsigned)NWORK) {
                __builtin_amdgcn_s_sleep(2);
                if (++spins > SPINCAP) { if (lane == 0) atomicAdd(tmo, 1u); break; }
            }
        }
        {
            __syncthreads();
            STAMP(3);
            bf16x8 of[4];
#pragma unroll
            for (int ks = 0; ks < 4; ++ks) of[ks] = *(const bf16x8*)(lds + L_OE + ((ks * 4 + g) * 8 + (i & 7)) * 16);
#pragma unroll
            for (int a = 0; a < TPW / 2; ++a) {
                if (a + 1 < TPW / 2) {
#pragma unroll
                    for (int f = 0; f < 8; ++f) wf[a + 1][f] = *(const bf16x8*)(wp + ((a + 1) * 8 + f) * 1024);
                }
                f32x4 d0, d1;
                AMFMA0(d0, wf[a][0], of[0]); AMFMA0(d1, wf[a][4], of[0]);
#pragma unroll
                for (int ks = 1; ks < 4; ++ks) { AMFMA(d0, wf[a][ks], of[ks]); AMFMA(d1, wf[a][4 + ks], of[ks]); }
                asm volatile("s_nop 7" : "+v"(d0), "+v"(d1));
                v4u v;
#pragma unroll
                for (int r = 0; r < 4; ++r) {
                    const float x1 = ror8(d1[r]);
                    v[r] = __float_as_uint(i < 8 ? d0[r] : x1) | 1u;
                }
                const int T = (jj * 4 + w) * TPW + 2 * a + (i >> 3);
                __builtin_amdgcn_raw_buffer_store_b128(v, prs, pofs + (unsigned)(h * 16384 + (4 * T + g) * 128 + (i & 7) * 16), 0, AUXS);
            }
        }
        STAMP(4);
        if (((s + 1) & 3) == 0) wait_kv((s + 1) >> 2);
        fetch_kv(s + 1, pk, pv);
        for (int z = 0; z < SLP1; ++z) __builtin_amdgcn_s_sleep(1);
        if (!is_red) {
            for (int z = 0; z < SLPN; ++z) __builtin_amdgcn_s_sleep(1);
        } else {
            v4u r[4];
            unsigned spins = 0;
            for (;;) {
#pragma unroll
                for (int q = 0; q < 4; ++q)
                    r[q] = __builtin_amdgcn_raw_buffer_load_b128(prs, pofs + (unsigned)((q * 8 + (lane >> 3)) * 16384 + red * 128 + (lane & 7) * 16), 0, AUXL);
                bool ok = true;
#pragma unroll
                for (int q = 0; q < 4; ++q) ok &= (r[q].x & r[q].y & r[q].z & r[q].w & 1u) != 0u;
                if (__all(ok)) break;
                if (++spins > SPINCAP) { if (lane == 0) atomicAdd(tmo, 1u); break; }
            }
            STAMP(5);
            float sm[4];
#pragma unroll
            for (int c = 0; c < 4; ++c) {
                sm[c] = (__uint_as_float(r[0][c] & ~1u) + __uint_as_float(r[1][c] & ~1u)) + (__uint_as_float(r[2][c] & ~1u) + __uint_as_float(r[3][c] & ~1u));
                sm[c] += ror8(sm[c]);
                sm[c] = xor16_add(sm[c]);
                sm[c] = xor32_add(sm[c]);
            }
            if (lane < 8) {
                v2u v = {pack2(sm[0], sm[1]), pack2(sm[2], sm[3])};
                if (v.x == 0u) v.x = 0x8000u;
                if (v.y == 0u) v.y = 0x8000u;
                __builtin_amdgcn_raw_buffer_store_b64(v, mrs, (unsigned)(s * 8192 + red * 64 + lane * 8), 0, AUXS);
            }
        }
        STAMP(6);
        for (int z = 0; z < SLP2; ++z) __builtin_amdgcn_s_sleep(1);
        {
            v4u m[2];
            unsigned spins = 0;
            for (;;) {
#pragma unroll
                for (int q = 0; q < 2; ++q) m[q] = __builtin_amdgcn_raw_buffer_load_b128(mrs, (unsigned)(s * 8192 + w * 2048 + q * 1024 + lane * 16), 0, AUXL);
                bool ok = true;
#pragma unroll
                for (int q = 0; q < 2; ++q) ok &= (m[q].x != 0u) & (m[q].y != 0u) & (m[q].z != 0u) & (m[q].w != 0u);
                if (__all(ok)) break;
                if (++spins > SPINCAP) { if (lane == 0) atomicAdd(tmo, 1u); break; }
            }
#pragma unroll
            for (int q = 0; q < 2; ++q) {
                const int R = w * 32 + q * 16 + (lane >> 2), t = 2 * (lane & 3);
                char* dst = lds + L_MEM + (R >> 3) * 512 + ((R >> 1) & 3) * 128 + t * 16 + (R & 1) * 8;
                *(v2u*)dst = (v2u){m[q].x, m[q].y};
                *(v2u*)(dst + 16) = (v2u){m[q].z, m[q].w};
            }
        }
        STAMP(7);
    }
}

__global__ __launch_bounds__(512, 2) void k_post(u16* QX, u16* KM, const bf16x8* __restrict__ WOT, f32x4* __restrict__ part) {
    __shared__ bf16x8 sa[4][16][64];
    const __amdgpu_buffer_rsrc_t qrs = __builtin_amdgcn_make_buffer_rsrc(QX, 0, 3u * QKV_MAT_BYTES, 0x00020000);
    const __amdgpu_buffer_rsrc_t krs = __builtin_amdgcn_make_buffer_rsrc(KM, 0, 2u * KM_BYTES, 0x00020000);
    const int w = __builtin_amdgcn_readfirstlane(threadIdx.x >> 6), lane = threadIdx.x & 63;
    const int kq = blockIdx.x & 7, mb = blockIdx.x >> 3;
    const bf16x8* pb = WOT + ((size_t)(kq * 4) * 32 + w * 4) * 4 * 64 + lane;
    bf16x8 b[4][4];
#pragma unroll
    for (int pk = 0; pk < 3; ++pk)
#pragma unroll
        for (int t = 0; t < 4; ++t) b[pk][t] = pb[(t * 4 + pk) * 64];
    xattn_unit(qrs, krs, &sa[(w >> 2) * 2][(w & 3) * 4][0], mb * 2 + (w >> 2), kq * 4 + (w & 3), lane);
    __syncthreads();
    f32x4 acc[4][4];
#pragma unroll
    for (int a = 0; a < 4; ++a)
#pragma unroll
        for (int t = 0; t < 4; ++t) acc[a][t] = (f32x4){0.f, 0.f, 0.f, 0.f};
#pragma unroll
    for (int kk = 0; kk < 16; ++kk) {
        if (kk + 3 < 16) {
            const int k2 = kk + 3;
#pragma unroll
            for (int t = 0; t < 4; ++t) b[k2 & 3][t] = pb[(((k2 >> 2) * 32 + t) * 4 + (k2 & 3)) * 64];
        }
        __builtin_amdgcn_sched_barrier(0);
        bf16x8 a[4];
#pragma unroll
        for (int mt = 0; mt < 4; ++mt) a[mt] = sa[mt][kk][lane];
#pragma unroll
        for (int mt = 0; mt < 4; ++mt)
#pragma unroll
            for (int t = 0; t < 4; ++t) acc[mt][t] = MFMA(b[kk & 3][t], a[mt], acc[mt][t]);
        __builtin_amdgcn_sched_barrier(0);
    }
#pragma unroll
    for (int mt = 0; mt < 4; ++mt)
#pragma unroll
        for (int t = 0; t < 4; ++t) part[(((size_t)kq * 128 + mb * 4 + mt) * 32 + w * 4 + t) * 64 + lane] = acc[mt][t];
}

__global__ __launch_bounds__(256) void k_out(const f32x4* __restrict__ part, const u16* __restrict__ Bout, const u16* __restrict__ x,
                                             u16* __restrict__ out) {
    const int id = blockIdx.x * 256 + threadIdx.x, lane = id & 63, tile = id >> 6, g = lane >> 4, i = lane & 15;
    constexpr size_t KS = (size_t)128 * 32 * 64;
    const f32x4 a = ((part[id] + part[KS + id]) + (part[2 * KS + id] + part[3 * KS + id])) +
                    ((part[4 * KS + id] + part[5 * KS + id]) + (part[6 * KS + id] + part[7 * KS + id]));
    const size_t idx = (size_t)((tile >> 5) * 16 + i) * DM + (tile & 31) * 16 + 4 * g;
    const v2u xr = *(const v2u*)(x + idx), br = *(const v2u*)(Bout + idx);
    const float xv[4] = {__uint_as_float(xr.x << 16), __uint_as_float(xr.x & 0xffff0000u), __uint_as_float(xr.y << 16), __uint_as_float(xr.y & 0xffff0000u)};
    const float bv[4] = {__uint_as_float(br.x << 16), __uint_as_float(br.x & 0xffff0000u), __uint_as_float(br.y << 16), __uint_as_float(br.y & 0xffff0000u)};
    float y[4];
#pragma unroll
    for (int r = 0; r < 4; ++r) y[r] = rbf(xv[r] + rbf(a[r])) + bv[r];
    *(v2u*)(out + idx) = (v2u){pack2(y[0], y[1]), pack2(y[2], y[3])};
}

extern "C" void kernel_launch(void* const* d_in, const int* in_sizes, int n_in, void* d_out, int out_size, void* d_ws, size_t ws_size,
                              hipStream_t stream) {
    const u16* x = (const u16*)d_in[0];
    const u16* mem0 = (const u16*)d_in[1];
    const u16* Wq = (const u16*)d_in[2];
    const u16* Wk = (const u16*)d_in[3];
    const u16* Wv = (const u16*)d_in[4];
    const u16* Wo = (const u16*)d_in[5];
    const u16* Wg = (const u16*)d_in[8];
    const u16* Wu = (const u16*)d_in[9];
    const u16* Wd = (const u16*)d_in[10];
    char* ws = (char*)d_ws;
    (void)hipFuncSetAttribute((const void*)k_chain, hipFuncAttributeMaxDynamicSharedMemorySize, L_TOTAL);
    k_prep<<<PREP_LN_BLOCKS + PREP_TILE_BLOCKS + PREP_ZERO_BLOCKS, 256, 0, stream>>>(x, Wq, Wk, Wv, Wo, Wg, Wu, Wd, ws);
    k_chain<<<NCH + NWORK, 256, L_TOTAL, stream>>>(mem0, (const bf16x8*)(ws + OFF_WT), (const bf16x8*)(ws + OFF_WOT), (u16*)(ws + OFF_KM), (u16*)(ws + OFF_VM), (unsigned*)(ws + OFF_P),
                                                  (unsigned*)(ws + OFF_M), (unsigned*)(ws + OFF_TMO), (const bf16x8*)(ws + OFF_XN),
                                                  (const bf16x8*)(ws + OFF_WG), (const bf16x8*)(ws + OFF_WU), (const bf16x8*)(ws + OFF_WD),
                                                  (u16*)(ws + OFF_QX), (u16*)(ws + OFF_H), Wg, Wu, Wd, ws, Wq, Wk, Wv, Wo);
    k_post<<<256, 512, 0, stream>>>((u16*)(ws + OFF_QX), (u16*)(ws + OFF_KM), (const bf16x8*)(ws + OFF_WOT), (f32x4*)(ws + OFF_PART));
    k_out<<<S_ * DM / 4 / 256, 256, 0, stream>>>((const f32x4*)(ws + OFF_PART), (const u16*)(ws + OFF_H), x, (u16*)d_out);
}
```
